```python
import torch

def _softmax(x, axis=-1):
    return torch.softmax(x, dim=axis)

def _gelu(x):
    return torch.nn.functional.gelu(x, approximate='tanh')

def _silu(x):
    return torch.nn.functional.silu(x)

def _sigmoid(x):
    return torch.sigmoid(x)

def _layernorm(x, axis=-1, eps=1e-05):
    m = x.mean(dim=axis, keepdim=True)
    v = x.var(dim=axis, keepdim=True, unbiased=False)
    return (x - m) / torch.sqrt(v + eps)

def _scatter_add(tgt, idx, src, axis=0):
    return tgt.clone().index_add_(axis, idx, src)

def _segment_sum(src, seg, n):
    out = torch.zeros((n,) + src.shape[1:], dtype=src.dtype)
    return out.index_add_(0, seg, src)

def _pad(x, pw):
    flat = tuple((v for pair in reversed(pw) for v in pair))
    return torch.nn.functional.pad(x, flat)
kernel_name = 'adaLN_cond__gqa_block_cached__dense_activation_relu__decode'

def setup_inputs():

    _w = lambda a: (lambda t: t.to(torch.bfloat16) if t.is_floating_point() else t)(torch.as_tensor(a))

    def _A_setup():
        B, Sq, D = (8, 1, 576)
        Hq, Hkv, Sk, Dh = (32, 16, 4096, 128)
        assert Hq % Hkv == 0, (Hq, Hkv)
        return {'x': _w((lambda *s: torch.randn(*s, dtype=torch.bfloat16) * 0.05)(B, Sq, D)), 'k_cache': _w((lambda *s: torch.randn(*s, dtype=torch.bfloat16) * 3.2)(B, Hkv, Sk, Dh)), 'v_cache': _w((lambda *s: torch.randn(*s, dtype=torch.bfloat16) * 0.4)(B, Hkv, Sk, Dh)), 'wq': _w((lambda *s: torch.randn(*s, dtype=torch.bfloat16) * 3.2)(D, Hq * Dh) * D ** (-0.5)), 'wo': _w((lambda *s: torch.randn(*s, dtype=torch.bfloat16) * 0.4)(Hq * Dh, D) * (Hq * Dh) ** (-0.5))}
    _A_in = _A_setup()

    def _B_setup():
        B, S, D, Dff = (8, 1, 576, 4608)
        return {'x': _w((lambda *s: torch.randn(*s, dtype=torch.bfloat16) * 0.05)(B, S, D)), 'w1': _w((lambda *s: torch.randn(*s, dtype=torch.bfloat16) * 0.05)(D, Dff)), 'w2': _w((lambda *s: torch.randn(*s, dtype=torch.bfloat16) * 0.05)(Dff, D))}
    _B_in = _B_setup()
    return {'A_x': _A_in['x'], 'A_k_cache': _A_in['k_cache'], 'A_v_cache': _A_in['v_cache'], 'A_wq': _A_in['wq'], 'A_wo': _A_in['wo'], 'B_x': _B_in['x'], 'B_w1': _B_in['w1'], 'B_w2': _B_in['w2'], 'c': _w((lambda *s: torch.randn(*s, dtype=torch.bfloat16) * 0.2)(8, 576) * 8.0), 'w_mod': _w((lambda *s: torch.randn(*s, dtype=torch.bfloat16) * 0.2)(576, 6 * 576) * 576 ** (-0.5) * 8.0)}

def reference(A_x, A_k_cache, A_v_cache, A_wq, A_wo, B_x, B_w1, B_w2, c, w_mod):

    def _A_ref(x, k_cache, v_cache, wq, wo):
        B, Sq, D = x.shape
        Hq, Dh = (32, 128)
        Hkv = k_cache.shape[1]
        rep = Hq // Hkv
        q = (lambda x, *d: x.permute(*d))((x @ wq).reshape(B, Sq, Hq, Dh), 0, 2, 1, 3)
        k = (lambda x, n, axis: torch.repeat_interleave(x, n, dim=axis))(k_cache, rep, axis=1)
        v = (lambda x, n, axis: torch.repeat_interleave(x, n, dim=axis))(v_cache, rep, axis=1)
        scores = torch.einsum('bhqd,bhkd->bhqk', q, k) * Dh ** (-0.5)
        p = _softmax(scores, axis=-1)
        o = torch.einsum('bhqk,bhkd->bhqd', p, v)
        return (lambda x, *d: x.permute(*d))(o, 0, 2, 1, 3).reshape(B, Sq, Hq * Dh) @ wo

    def _B_ref(x, w1, w2):
        h = torch.einsum('bsd,df->bsf', x, w1)
        zero = torch.zeros_like(h)
        h = torch.maximum(h, zero)
        return torch.einsum('bsf,fd->bsd', h, w2)
    x = A_x
    mod = (_silu(c) @ w_mod).reshape(c.shape[0], 6, 1, -1)
    sa, ca, ga, sb, cb, gb = (mod[:, 0].clone(), mod[:, 1].clone(), mod[:, 2].clone(), mod[:, 3].clone(), mod[:, 4].clone(), mod[:, 5].clone())
    h = _layernorm(x, axis=-1) * (1 + ca) + sa
    x = x + ga * _A_ref(h, A_k_cache, A_v_cache, A_wq, A_wo)
    h = _layernorm(x, axis=-1) * (1 + cb) + sb
    x = x + gb * _B_ref(h, B_w1, B_w2)
    return x
```

```cpp
#include <hip/hip_runtime.h>
#include <stdint.h>

#define MIMIC 1
#define RING 3
#define PACE 4

typedef unsigned u32;
using u32x2 = __attribute__((ext_vector_type(2))) unsigned;
using u32x4 = __attribute__((ext_vector_type(4))) unsigned;
using f32x2 = __attribute__((ext_vector_type(2))) float;
using f32x4 = __attribute__((ext_vector_type(4))) float;
using s16x4 = __attribute__((ext_vector_type(4))) short;
using bf16x2 = __attribute__((ext_vector_type(2))) __bf16;
using bf16x8 = __attribute__((ext_vector_type(8))) __bf16;
typedef __amdgpu_buffer_rsrc_t rsrc_t;
#define LAS __attribute__((address_space(3)))
#define DI __device__ __forceinline__

constexpr int NWG = 256, NTHR = 576;
constexpr int DM = 576, NMOD = 3456, NQ = 4096, DFF = 4608;
constexpr int AUX_NT = 2, AUX_SC1 = 16;
constexpr u32 SENT = 0xFFFFFFFFu;
constexpr u32 SPIN_CAP = 1u << 16;

constexpr u32 WS_EPOCH = 0;
constexpr u32 WS_HG = 4096;
constexpr u32 WS_MG = WS_HG + 18432;
constexpr u32 WS_QG = WS_MG + 73728;
constexpr u32 WS_CLR = WS_QG + 131072;
constexpr u32 WS_OP = WS_CLR;
constexpr u32 WS_ML = WS_OP + 131072;
constexpr u32 WS_X2 = WS_ML + 2048;
constexpr u32 WS_FP = WS_CLR + 36 * 5120;
constexpr u32 WS_END = WS_FP + 144 * 18432;
constexpr u32 WS_STAMP = (WS_END + 4095) & ~4095u;
static_assert(WS_X2 + 36864 <= WS_FP, "cleared region");

constexpr u32 L_WM = 0;
constexpr u32 L_WQ = 36864;
constexpr u32 L_WT = 55296;
constexpr u32 L_RED = 129024;
constexpr u32 L_ST = L_RED + 9216;
constexpr u32 L_Q = L_ST + 64;
constexpr u32 L_END = L_Q + 512;
constexpr u32 L_MOD = 0;
constexpr u32 L_GB = 27648;
constexpr u32 L_XR = 27648 + 64;
constexpr u32 L_AO = 28160;
constexpr u32 L_AM = L_AO + 8192;
constexpr u32 L_H2 = 36864;
constexpr u32 H2S = 1168;

struct Params {
  const void* x; const void* kc; const void* vc; const void* wq; const void* wo;
  const void* w1; const void* w2; const void* c; const void* wm; void* out; char* ws;
};

DI rsrc_t mk(const void* p, u32 n) { return __builtin_amdgcn_make_buffer_rsrc((void*)p, 0, n, 0x00020000); }
template <int AUX> DI u32x4 ld128(rsrc_t r, u32 v, u32 s) { return __builtin_amdgcn_raw_buffer_load_b128(r, v, s, AUX); }
template <int AUX> DI u32x2 ld64(rsrc_t r, u32 v, u32 s) { return __builtin_amdgcn_raw_buffer_load_b64(r, v, s, AUX); }
template <int AUX> DI u32 ld32(rsrc_t r, u32 v, u32 s) { return __builtin_amdgcn_raw_buffer_load_b32(r, v, s, AUX); }
DI void st128(rsrc_t r, u32x4 d, u32 v, u32 s) { __builtin_amdgcn_raw_buffer_store_b128(d, r, v, s, AUX_SC1); }
DI void st32(rsrc_t r, u32 d, u32 v, u32 s) { __builtin_amdgcn_raw_buffer_store_b32(d, r, v, s, AUX_SC1); }
DI void dma(rsrc_t r, u32 v, u32 s, u32 lds) {
  u32 keep;
  asm volatile("s_mov_b32 %0, m0\n\ts_mov_b32 m0, %4\n\ts_nop 3\n\tbuffer_load_dwordx4 %1, %2, %3 offen nt lds\n\ts_mov_b32 m0, %0"
               : "=&s"(keep) : "v"(v), "s"(r), "s"(s), "s"(lds) : "memory");
}
#define VMCNT(n) asm volatile("s_waitcnt vmcnt(" #n ")" ::: "memory")
template <int N> DI void vmcnt() { asm volatile("s_waitcnt vmcnt(%0)" ::"i"(N) : "memory"); }
DI void bar() { asm volatile("s_waitcnt lgkmcnt(0)\n\ts_barrier" ::: "memory"); }
#define SCHED() __builtin_amdgcn_sched_barrier(0)

DI u32 pk(float lo, float hi) { f32x2 v = {lo, hi}; return __builtin_bit_cast(u32, __builtin_convertvector(v, bf16x2)); }
DI float blo(u32 u) { return __builtin_bit_cast(float, u << 16); }
DI float bhi(u32 u) { return __builtin_bit_cast(float, u & 0xffff0000u); }
DI float rb(float x) { return blo(pk(x, x)); }
DI float asf(u32 u) { return __builtin_bit_cast(float, u); }
DI u32 asu(float f) { return __builtin_bit_cast(u32, f); }
DI u32 nosent(u32 u) { return u == SENT ? 0x7FC07FC0u : u; }
template <int CTRL> DI u32 dppu(u32 x) { return __builtin_amdgcn_mov_dpp(x, CTRL, 0xf, 0xf, true); }
template <int CTRL> DI float dppf(float x) { return asf(dppu<CTRL>(asu(x))); }
constexpr int XOR1 = 0xB1, XOR2 = 0x4E, HMIR = 0x141, ROR8 = 0x128;
DI float hi8(float a, float b) { return asf(__builtin_amdgcn_update_dpp(asu(a), asu(b), ROR8, 0xf, 0xc, false)); }
DI float xg_max(float x) {
  auto s = __builtin_amdgcn_permlane16_swap(asu(x), asu(x), false, false);
  x = fmaxf(asf(s[0]), asf(s[1]));
  auto t = __builtin_amdgcn_permlane32_swap(asu(x), asu(x), false, false);
  return fmaxf(asf(t[0]), asf(t[1]));
}
DI float xg_sum(float x) {
  auto s = __builtin_amdgcn_permlane16_swap(asu(x), asu(x), false, false);
  x = asf(s[0]) + asf(s[1]);
  auto t = __builtin_amdgcn_permlane32_swap(asu(x), asu(x), false, false);
  return asf(t[0]) + asf(t[1]);
}
DI float wave_sum(float x) {
  x += dppf<XOR1>(x); x += dppf<XOR2>(x); x += dppf<HMIR>(x); x += dppf<ROR8>(x);
  return xg_sum(x);
}
DI f32x4 mfma(u32x4 a, u32x4 b, f32x4 c) {
  return __builtin_amdgcn_mfma_f32_16x16x32_bf16(__builtin_bit_cast(bf16x8, a), __builtin_bit_cast(bf16x8, b), c, 0, 0, 0);
}
DI u32x2 trd(LAS char* p) { return __builtin_bit_cast(u32x2, __builtin_amdgcn_ds_read_tr16_b64_v4i16((LAS s16x4*)p)); }
DI u32x4 trfrag(LAS char* p, u32 second) { u32x2 a = trd(p), b = trd(p + second); return u32x4{a[0], a[1], b[0], b[1]}; }
DI float silu(float x) { return x * __builtin_amdgcn_rcpf(1.f + __builtin_amdgcn_exp2f(-1.4426950408889634f * x)); }
DI bool has_sent(u32x4 v) { return (v[0] == SENT) | (v[1] == SENT) | (v[2] == SENT) | (v[3] == SENT); }

DI void ln_stats(const float (&v)[12], bool tail, float& mean_b, float& den_b) {
  float s = 0.f;
#pragma unroll
  for (int i = 0; i < 8; ++i) s += v[i];
  float s2 = (v[8] + v[9]) + (v[10] + v[11]);
  s += tail ? s2 : 0.f;
  const float mean = wave_sum(s) * (1.f / DM);
  float q = 0.f, q2 = 0.f;
#pragma unroll
  for (int i = 0; i < 8; ++i) { const float d = v[i] - mean; q = fmaf(d, d, q); }
#pragma unroll
  for (int i = 8; i < 12; ++i) { const float d = v[i] - mean; q2 = fmaf(d, d, q2); }
  q += tail ? q2 : 0.f;
  const float var = wave_sum(q) * (1.f / DM);
  mean_b = rb(mean);
  den_b = rb(__builtin_sqrtf(rb(rb(var) + 1e-5f)));
}
DI float ln_mod(float x, float mean_b, float den_b, float cc, float ss) {
  const float n = rb(rb(x - mean_b) / den_b);
  return rb(n * rb(1.f + cc)) + ss;
}

#define TS(i) do {} while (0)

__global__ void __launch_bounds__(NTHR, 3) fused_block(Params p) {
  extern __shared__ __attribute__((aligned(16))) char smem_[];
  LAS char* sm = (LAS char*)smem_;
  const u32 lds0 = (u32)(uintptr_t)sm;
  const int tid = threadIdx.x, lane = tid & 63, w = __builtin_amdgcn_readfirstlane(tid >> 6);
  const int bid = blockIdx.x;
  const int g = lane >> 4, n = lane & 15;
  const int G = (bid >> 5) * 8 + (bid & 7), r4 = (bid >> 3) & 3;
  const bool roleM = G < 27, roleO = G >= 10 && G < 28, roleF = G >= 28;
  const int mkind = G / 9;
  const int md0 = (G - mkind * 9) * 64 + r4 * 16;
  const rsrc_t rws = mk(p.ws, WS_END);
  TS(0);
  const u32x2 ep = *(const u32x2*)(p.ws + WS_EPOCH);
  const u32 tag0 = ep[0] + 1u, tag1 = ep[1] + 1u;
  const u32 frow = ((lane >> 3) & 3) * 8 + (lane >> 5) * 4 + ((lane >> 1) & 3);

  const rsrc_t rk = mk((const char*)p.kc + (size_t)bid * 524288, 524288);
  const rsrc_t rv = mk((const char*)p.vc + (size_t)bid * 524288, 524288);
  const u32 koff = n * 256 + g * 16, voff = g * 1024 + n * 16;
  u32x4 KB[RING][4], VB[RING][4];
#define LOADK(slot, t) _Pragma("unroll") for (int i_ = 0; i_ < 4; ++i_) KB[slot][i_] = ld128<AUX_NT>(rk, koff + i_ * 64, ((t) * 8 + w) * 4096)
#define LOADV(slot, t) _Pragma("unroll") for (int i_ = 0; i_ < 4; ++i_) VB[slot][i_] = ld128<AUX_NT>(rv, voff + i_ * 256, ((t) * 8 + w) * 4096)

  u32x2 xq = {0u, 0u};
  if (roleM) {
    const rsrc_t rc = mk(p.c, 8 * DM * 2), rm = mk(p.wm, DM * NMOD * 2), rx = mk(p.x, 8 * DM * 2);
    const int colA = (mkind == 0 ? 0 : mkind == 1 ? 1728 : 1152) + md0, colB = (mkind == 0 ? 576 : mkind == 1 ? 2304 : 2880) + md0;
    u32x2 cv[2], xv[3];
#pragma unroll
    for (int i = 0; i < 2; ++i) cv[i] = ld64<0>(rc, (n & 7) * 1152 + g * 16 + (n >> 3) * 8, (2 * w + i) * 64);
    if (mkind == 0) {
      if (w < 8) {
#pragma unroll
        for (int i = 0; i < 3; ++i) xv[i] = ld64<0>(rx, (i < 2 || lane < 16) ? lane * 8 + i * 512 : 0, w * 1152);
        if (w < 5) st128(rws, u32x4{SENT, SENT, SENT, SENT}, lane * 16, WS_CLR + (G * 4 + r4) * 5120 + w * 1024);
      } else {
        xq = ld64<0>(rx, (n & 7) * 1152 + g * 8, md0 * 2);
      }
    }
    SCHED();
    const u32 mv = frow * (NMOD * 2) + (lane & 1) * 16;
#pragma unroll
    for (int i = 0; i < 2; ++i) {
      const int s = 2 * w + i;
      dma(rm, mv, s * 32 * NMOD * 2 + colA * 2, lds0 + L_WM + s * 1024);
      dma(rm, mv, s * 32 * NMOD * 2 + colB * 2, lds0 + L_WM + 18432 + s * 1024);
    }
    SCHED();
    u32x4 bf[2];
#pragma unroll
    for (int i = 0; i < 2; ++i) {
      const u32 a = pk(silu(blo(cv[i][0])), silu(bhi(cv[i][0]))), b = pk(silu(blo(cv[i][1])), silu(bhi(cv[i][1])));
      bf[i] = u32x4{a, b, dppu<ROR8>(a), dppu<ROR8>(b)};
    }
    if (mkind == 0 && w < 8) {
      float v[12];
#pragma unroll
      for (int i = 0; i < 3; ++i) { v[4 * i] = blo(xv[i][0]); v[4 * i + 1] = bhi(xv[i][0]); v[4 * i + 2] = blo(xv[i][1]); v[4 * i + 3] = bhi(xv[i][1]); }
      float mb, db;
      ln_stats(v, lane < 16, mb, db);
      if (lane == 0) *(LAS f32x2*)(sm + L_ST + w * 8) = f32x2{mb, db};
    }
    VMCNT(0);
    TS(1);
    f32x4 dA = {0.f, 0.f, 0.f, 0.f}, dB = dA;
#pragma unroll
    for (int i = 0; i < 2; ++i) {
      LAS char* a = sm + L_WM + (2 * w + i) * 1024 + lane * 8;
      dA = mfma(trfrag(a, 512), bf[i], dA);
      dB = mfma(trfrag(a + 18432, 512), bf[i], dB);
    }
    *(LAS f32x4*)(sm + L_RED + w * 1024 + lane * 16) = f32x4{hi8(dA[0], dB[0]), hi8(dA[1], dB[1]), hi8(dA[2], dB[2]), hi8(dA[3], dB[3])};
    bar();
  }

  float am = -INFINITY, al = 0.f;
  f32x4 acc = {0.f, 0.f, 0.f, 0.f};

  if (w == 8) {
    const int qt = G * 4 + r4;
    {
      const rsrc_t rq = mk(p.wq, DM * NQ * 2);
      const u32 qv = frow * (NQ * 2) + (lane & 1) * 16;
      if (roleM) {
        f32x4 s = *(LAS f32x4*)(sm + L_RED + lane * 16);
#pragma unroll
        for (int i = 1; i < 9; ++i) s += *(LAS f32x4*)(sm + L_RED + i * 1024 + lane * 16);
        u32 a = pk(s[0], s[1]), b = pk(s[2], s[3]);
        if (mkind == 0) {
          const u32 ca = dppu<ROR8>(a), cb = dppu<ROR8>(b);
          const f32x2 st = *(LAS f32x2*)(sm + L_ST + (n & 7) * 8);
          const u32 h0 = pk(ln_mod(blo(xq[0]), st[0], st[1], blo(ca), blo(a)), ln_mod(bhi(xq[0]), st[0], st[1], bhi(ca), bhi(a)));
          const u32 h1 = pk(ln_mod(blo(xq[1]), st[0], st[1], blo(cb), blo(b)), ln_mod(bhi(xq[1]), st[0], st[1], bhi(cb), bhi(b)));
          if (n < 8) st128(rws, u32x4{h0, tag0, h1, tag1}, (g * 8 + n) * 16, WS_HG + md0 * 32);
        } else {
          st128(rws, u32x4{a, tag0, b, tag1}, (n >> 3) * 18432 + (n & 7) * 2304 + g * 16, WS_MG + (mkind - 1) * 36864 + md0 * 4);
        }
        TS(2);
      }
#pragma unroll
      for (int s = 0; s < 18; ++s) dma(rq, qv, s * 32 * NQ * 2 + qt * 32, lds0 + L_WQ + s * 1024);
    }
    u32x4 hg[18];
    for (u32 spin = 0;; ++spin) {
      asm volatile("" ::: "memory");
#pragma unroll
      for (int s = 0; s < 18; ++s) hg[s] = ld128<AUX_SC1>(rws, lane * 16, WS_HG + s * 1024);
      bool ok = true;
#pragma unroll
      for (int s = 0; s < 18; ++s) ok &= (hg[s][1] == tag0) & (hg[s][3] == tag1);
      if (__all(ok) || spin > SPIN_CAP) break;
    }
    VMCNT(0);
    TS(3);
    {
      f32x4 d0 = {0.f, 0.f, 0.f, 0.f}, d1 = d0;
#pragma unroll
      for (int s = 0; s < 18; s += 2) {
        d0 = mfma(trfrag(sm + L_WQ + s * 1024 + lane * 8, 512), u32x4{hg[s][0], hg[s][2], dppu<ROR8>(hg[s][0]), dppu<ROR8>(hg[s][2])}, d0);
        d1 = mfma(trfrag(sm + L_WQ + (s + 1) * 1024 + lane * 8, 512), u32x4{hg[s + 1][0], hg[s + 1][2], dppu<ROR8>(hg[s + 1][0]), dppu<ROR8>(hg[s + 1][2])}, d1);
      }
      d0 += d1;
      if (n < 8) st128(rws, u32x4{pk(d0[0], d0[1]), tag0, pk(d0[2], d0[3]), tag1}, n * 16384 + g * 16, WS_QG + qt * 64);
    }
    TS(4);
    {
      u32x4 qg;
      for (u32 spin = 0;; ++spin) {
        asm volatile("" ::: "memory");
        qg = ld128<AUX_SC1>(rws, lane * 16, WS_QG + (bid >> 1) * 1024);
        if (__all((qg[1] == tag0) & (qg[3] == tag1)) || spin > SPIN_CAP) break;
      }
      *(LAS u32x2*)(sm + L_Q + lane * 8) = u32x2{qg[0], qg[2]};
    }
    TS(5);
    bar();
    if (roleO) {
      const int oi = (G - 10) * 4 + r4, ks = oi / 36, ct = oi - ks * 36;
      const rsrc_t ro = mk(p.wo, NQ * DM * 2);
      const u32 ov = frow * (DM * 2) + (lane & 1) * 16;
      for (int s = 0; s < 64; s += 4) {
#pragma unroll
        for (int i = 0; i < 4; ++i) dma(ro, ov, (ks * 64 + s + i) * 32 * DM * 2 + ct * 32, lds0 + L_WT + (s + i) * 1024);
        VMCNT(4);
      }
    }
    if (roleF) {
      const int j = (G - 28) * 4 + r4;
      const rsrc_t r1 = mk(p.w1, DM * DFF * 2), r2 = mk(p.w2, DFF * DM * 2);
#pragma unroll
      for (int i = 0; i < 18; ++i) st128(rws, u32x4{SENT, SENT, SENT, SENT}, lane * 16, WS_FP + j * 18432 + i * 1024);
      const u32 v1 = (g * 8 + ((lane >> 2) & 3)) * (DFF * 2) + (lane & 3) * 16;
      for (int s = 0; s < 18; ++s) {
        dma(r1, v1, (s * 32) * DFF * 2 + j * 64, lds0 + L_WT + s * 2048);
        dma(r1, v1, (s * 32 + 4) * DFF * 2 + j * 64, lds0 + L_WT + s * 2048 + 1024);
        dma(r2, lane * 16, j * 36864 + s * 2048, lds0 + L_WT + 36864 + s * 2048);
        dma(r2, lane * 16, j * 36864 + s * 2048 + 1024, lds0 + L_WT + 36864 + s * 2048 + 1024);
        VMCNT(4);
      }
      for (int c6 = 0; c6 < 9; ++c6) {
        u32x4 m[6];
        for (u32 spin = 0;; ++spin) {
          asm volatile("" ::: "memory");
          bool ok = true;
#pragma unroll
          for (int i = 0; i < 6; ++i) { m[i] = ld128<AUX_SC1>(rws, lane * 16, WS_MG + (c6 * 6 + i) * 1024); }
#pragma unroll
          for (int i = 0; i < 6; ++i) ok &= (m[i][1] == tag0) & (m[i][3] == tag1);
          if (__all(ok) || spin > SPIN_CAP) break;
        }
#pragma unroll
        for (int i = 0; i < 6; ++i) *(LAS u32x2*)(sm + L_MOD + (c6 * 6 + i) * 512 + lane * 8) = u32x2{m[i][0], m[i][2]};
      }
      {
        u32x4 m;
        for (u32 spin = 0;; ++spin) {
          asm volatile("" ::: "memory");
          m = ld128<AUX_SC1>(rws, (lane & 7) * 16, WS_MG + 3 * 18432 + j * 128);
          if (__all((m[1] == tag0) & (m[3] == tag1)) || spin > SPIN_CAP) break;
        }
        if (lane < 8) *(LAS u32x2*)(sm + L_GB + lane * 8) = u32x2{m[0], m[2]};
      }
    }
    VMCNT(0);
    TS(6);
  } else {
#pragma unroll
    for (int t = 0; t < RING; ++t) {
      LOADK(t, t);
      SCHED(); if (t > 0 || PACE < 8) vmcnt<PACE>(); SCHED();
      LOADV(t, t);
      SCHED(); if (t + 1 < RING) vmcnt<PACE>(); SCHED();
    }
    TS(2);
    bar();
    TS(5);
    u32x4 qf[4];
#pragma unroll
    for (int i = 0; i < 4; ++i) qf[i] = *(LAS u32x4*)(sm + L_Q + (n >> 3) * 256 + i * 64 + g * 16);
    u32 pm[4];
#pragma unroll
    for (int i = 0; i < 4; ++i) pm[i] = ((n & 7) >> 1) == i ? ((n & 1) ? 0xffff0000u : 0x0000ffffu) : 0u;
#pragma unroll
    for (int t = 0; t < 16; ++t) {
      const int sl = t % RING;
      f32x4 s = {0.f, 0.f, 0.f, 0.f};
#pragma unroll
      for (int i = 0; i < 4; ++i) s = mfma(KB[sl][i], qf[i], s);
      SCHED();
      if (t + RING < 16) { LOADK(sl, t + RING); }
      SCHED();
      constexpr float SC = 0.08838834764831845f, L2E = 1.4426950408889634f;
      float e[4];
      {
        const u32 a = pk(s[0], s[1]), b = pk(s[2], s[3]);
        const u32 a2 = pk(blo(a) * SC, bhi(a) * SC), b2 = pk(blo(b) * SC, bhi(b) * SC);
        e[0] = blo(a2); e[1] = bhi(a2); e[2] = blo(b2); e[3] = bhi(b2);
      }
      const float mx = xg_max(fmaxf(fmaxf(e[0], e[1]), fmaxf(e[2], e[3])));
      const float mn = fmaxf(am, mx);
      const float alpha = __builtin_amdgcn_exp2f((am - mn) * L2E);
      am = mn;
#pragma unroll
      for (int i = 0; i < 4; ++i) e[i] = __builtin_amdgcn_exp2f((e[i] - mn) * L2E);
      al = fmaf(al, alpha, (e[0] + e[1]) + (e[2] + e[3]));
      acc *= alpha;
#pragma unroll
      for (int i = 0; i < 4; ++i) {
        const u32 pb = pk(e[i], e[i]);
        acc = mfma(VB[sl][i], u32x4{pb & pm[0], pb & pm[1], pb & pm[2], pb & pm[3]}, acc);
      }
      SCHED();
      if (t + RING < 16) { LOADV(sl, t + RING); }
      SCHED();
    }
    al = xg_sum(al);
#pragma unroll
    for (int i = 0; i < 4; ++i) *(LAS float*)(sm + L_AO + w * 1024 + ((n >> 3) * 128 + g * 32 + i * 8 + (n & 7)) * 4) = acc[i];
    if (g == 0 && (n & 7) == 0) *(LAS f32x2*)(sm + L_AM + (n >> 3) * 64 + w * 8) = f32x2{am, al};
    VMCNT(0);
    TS(6);
  }
  bar();
  if (w < 2) {
    constexpr float L2E = 1.4426950408889634f;
    f32x4 ml[4];
#pragma unroll
    for (int i = 0; i < 4; ++i) ml[i] = *(LAS f32x4*)(sm + L_AM + w * 64 + i * 16);
    f32x2 o[8];
#pragma unroll
    for (int i = 0; i < 8; ++i) o[i] = *(LAS f32x2*)(sm + L_AO + i * 1024 + w * 512 + lane * 8);
    float M = ml[0][0];
#pragma unroll
    for (int i = 1; i < 8; ++i) M = fmaxf(M, ml[i >> 1][(i & 1) * 2]);
    float L = 0.f, o0 = 0.f, o1 = 0.f;
#pragma unroll
    for (int i = 0; i < 8; ++i) {
      const float a = __builtin_amdgcn_exp2f((ml[i >> 1][(i & 1) * 2] - M) * L2E);
      L = fmaf(a, ml[i >> 1][(i & 1) * 2 + 1], L); o0 = fmaf(a, o[i][0], o0); o1 = fmaf(a, o[i][1], o1);
    }
    const float inv = 1.f / L;
    const int slot = (((bid >> 1) & 15) * 2 + w) * 16 + (bid & 1) * 8 + (bid >> 5);
    st32(rws, nosent(pk(o0 * inv, o1 * inv)), lane * 4, WS_OP + slot * 256);
    if (lane == 0) st32(rws, nosent(asu(fmaf(M, L2E, __builtin_amdgcn_logf(L)))), 0, WS_ML + slot * 4);
  }
  TS(7);

  if (roleO) {
    const int oi = (G - 10) * 4 + r4, ks = oi / 36, ct = oi - ks * 36;
    if (w < 8) {
      u32x4 wf[8], ob[8]; u32 ls[2];
#pragma unroll
      for (int i = 0; i < 8; ++i) wf[i] = trfrag(sm + L_WT + (w * 8 + i) * 1024 + lane * 8, 512);
      const int h0 = ks * 16 + w * 2;
      for (u32 spin = 0;; ++spin) {
        asm volatile("" ::: "memory");
#pragma unroll
        for (int i = 0; i < 8; ++i) ob[i] = ld128<AUX_SC1>(rws, n * 256 + g * 16 + (i & 3) * 64, WS_OP + (h0 + (i >> 2)) * 4096);
#pragma unroll
        for (int i = 0; i < 2; ++i) ls[i] = ld32<AUX_SC1>(rws, n * 4, WS_ML + (h0 + i) * 64);
        bool bad = (ls[0] == SENT) | (ls[1] == SENT);
#pragma unroll
        for (int i = 0; i < 8; ++i) bad |= has_sent(ob[i]);
        if (!__any(bad) || spin > SPIN_CAP) break;
      }
      TS(8);
      f32x4 d0 = {0.f, 0.f, 0.f, 0.f}, d1 = d0;
#pragma unroll
      for (int i = 0; i < 4; ++i) { d0 = mfma(wf[i], ob[i], d0); d1 = mfma(wf[4 + i], ob[4 + i], d1); }
      const float a0 = 1.f / (1.f + __builtin_amdgcn_exp2f(dppf<ROR8>(asf(ls[0])) - asf(ls[0])));
      const float a1 = 1.f / (1.f + __builtin_amdgcn_exp2f(dppf<ROR8>(asf(ls[1])) - asf(ls[1])));
      *(LAS f32x4*)(sm + L_RED + w * 1024 + lane * 16) = d0 * a0 + d1 * a1;
    }
    bar();
    if (w == 0) {
      f32x4 s = *(LAS f32x4*)(sm + L_RED + lane * 16);
#pragma unroll
      for (int i = 1; i < 8; ++i) s += *(LAS f32x4*)(sm + L_RED + i * 1024 + lane * 16);
      u32x4 o;
#pragma unroll
      for (int i = 0; i < 4; ++i) o[i] = nosent(asu(s[i] + dppf<ROR8>(s[i])));
      if (n < 8) st128(rws, o, n * 2304 + g * 16, WS_X2 + ks * 18432 + ct * 64);
      TS(9);
    }
  }

  if (roleF) {
    const int j = (G - 28) * 4 + r4, jb = j / 18, jc = j - jb * 18;
    u32x4 w1f[4], w2f[4];
#pragma unroll
    for (int i = 0; i < 4; ++i) {
      w1f[i] = trfrag(sm + L_WT + (2 * w + (i >> 1)) * 2048 + (lane >> 2) * 64 + (i & 1) * 32 + (lane & 3) * 8, 1024);
      w2f[i] = trfrag(sm + L_WT + 36864 + (lane >> 2) * 1152 + (4 * w + i) * 32 + (lane & 3) * 8, 16 * 1152);
    }
    if (w < 8) {
      const rsrc_t rx = mk(p.x, 8 * DM * 2);
      const bool tail = lane < 16;
      u32x2 xv[3], sb[3], cb[3], ga[3];
#pragma unroll
      for (int i = 0; i < 3; ++i) {
        const u32 o = (i < 2 || tail) ? lane * 8 + i * 512 : 0;
        xv[i] = ld64<0>(rx, o, w * 1152);
        sb[i] = *(LAS u32x2*)(sm + L_MOD + w * 1152 + o);
        cb[i] = *(LAS u32x2*)(sm + L_MOD + 9216 + w * 1152 + o);
        ga[i] = *(LAS u32x2*)(sm + L_MOD + 18432 + w * 1152 + o);
      }
      u32x4 pa[6];
      for (u32 spin = 0;; ++spin) {
        asm volatile("" ::: "memory");
        bool bad = false;
#pragma unroll
        for (int i = 0; i < 6; ++i) pa[i] = ld128<AUX_SC1>(rws, ((i >> 1) < 2 || tail) ? lane * 16 + (i >> 1) * 1024 : 0, WS_X2 + (i & 1) * 18432 + w * 2304);
#pragma unroll
        for (int i = 0; i < 6; ++i) bad |= has_sent(pa[i]);
        if (!__any(bad) || spin > SPIN_CAP) break;
      }
      TS(10);
      float v[12];
#pragma unroll
      for (int i = 0; i < 3; ++i)
#pragma unroll
        for (int k = 0; k < 4; ++k) {
          const float at = rb(asf(pa[2 * i][k]) + asf(pa[2 * i + 1][k]));
          const float gg = (k & 1) ? bhi(ga[i][k >> 1]) : blo(ga[i][k >> 1]), xx = (k & 1) ? bhi(xv[i][k >> 1]) : blo(xv[i][k >> 1]);
          v[4 * i + k] = rb(xx + rb(gg * at));
        }
      float mb, db;
      ln_stats(v, tail, mb, db);
#pragma unroll
      for (int i = 0; i < 3; ++i) {
        const u32 a = pk(ln_mod(v[4 * i], mb, db, blo(cb[i][0]), blo(sb[i][0])), ln_mod(v[4 * i + 1], mb, db, bhi(cb[i][0]), bhi(sb[i][0])));
        const u32 b = pk(ln_mod(v[4 * i + 2], mb, db, blo(cb[i][1]), blo(sb[i][1])), ln_mod(v[4 * i + 3], mb, db, bhi(cb[i][1]), bhi(sb[i][1])));
        if (i < 2 || tail) *(LAS u32x2*)(sm + L_H2 + w * H2S + lane * 8 + i * 512) = u32x2{a, b};
        const int q = lane + 64 * i - jc * 8;
        if (w == jb && q >= 0 && q < 8 && (i < 2 || tail)) *(LAS f32x4*)(sm + L_XR + q * 16) = f32x4{v[4 * i], v[4 * i + 1], v[4 * i + 2], v[4 * i + 3]};
      }
    }
    bar();
    {
      f32x4 d0 = {0.f, 0.f, 0.f, 0.f}, d1 = d0;
#pragma unroll
      for (int i = 0; i < 2; ++i) {
        const u32x4 hb = *(LAS u32x4*)(sm + L_H2 + (n & 7) * H2S + (2 * w + i) * 64 + g * 16);
        d0 = mfma(w1f[2 * i], hb, d0); d1 = mfma(w1f[2 * i + 1], hb, d1);
      }
      *(LAS f32x4*)(sm + L_RED + w * 1024 + lane * 16) = n < 8 ? d0 : d1;
    }
    bar();
    {
      f32x4 s = *(LAS f32x4*)(sm + L_RED + lane * 16);
#pragma unroll
      for (int i = 1; i < 9; ++i) s += *(LAS f32x4*)(sm + L_RED + i * 1024 + lane * 16);
      const u32 a = pk(s[0] < 0.f ? 0.f : s[0], s[1] < 0.f ? 0.f : s[1]), b = pk(s[2] < 0.f ? 0.f : s[2], s[3] < 0.f ? 0.f : s[3]);
      const u32 a2 = dppu<ROR8>(a), b2 = dppu<ROR8>(b);
      const u32x4 hb = n < 8 ? u32x4{a, b, a2, b2} : u32x4{a2, b2, a, b};
      const f32x4 z = {0.f, 0.f, 0.f, 0.f};
#pragma unroll
      for (int i = 0; i < 2; ++i) {
        const f32x4 e0 = mfma(w2f[2 * i], hb, z), e1 = mfma(w2f[2 * i + 1], hb, z);
        const f32x4 e = n < 8 ? e0 : e1;
        st128(rws, u32x4{nosent(asu(e[0])), nosent(asu(e[1])), nosent(asu(e[2])), nosent(asu(e[3]))},
              (n & 7) * (18 * 18432) + (n >> 3) * 64 + g * 16, WS_FP + (2 * w + i) * 18432 + j * 128);
      }
    }
    TS(11);
    u32x4 fa[2];
    for (u32 spin = 0;; ++spin) {
      asm volatile("" ::: "memory");
#pragma unroll
      for (int i = 0; i < 2; ++i) fa[i] = ld128<AUX_SC1>(rws, lane * 16, WS_FP + j * 18432 + (2 * w + i) * 1024);
      if (!__any(has_sent(fa[0]) | has_sent(fa[1])) || spin > SPIN_CAP) break;
    }
    TS(12);
    f32x4 s;
#pragma unroll
    for (int i = 0; i < 4; ++i) { float t = asf(fa[0][i]) + asf(fa[1][i]); t += dppf<ROR8>(t); s[i] = xg_sum(t); }
    if (lane < 8) *(LAS f32x4*)(sm + L_AO + w * 128 + lane * 16) = s;
    bar();
    if (w == 0 && lane < 16) {
      f32x2 t = *(LAS f32x2*)(sm + L_AO + lane * 8);
#pragma unroll
      for (int i = 1; i < 9; ++i) t += *(LAS f32x2*)(sm + L_AO + i * 128 + lane * 8);
      const u32 gb = *(LAS u32*)(sm + L_GB + lane * 4);
      const f32x2 xr = *(LAS f32x2*)(sm + L_XR + lane * 8);
      ((u32*)p.out)[j * 16 + lane] = pk(xr[0] + rb(blo(gb) * rb(t[0])), xr[1] + rb(bhi(gb) * rb(t[1])));
      if (j == 0 && lane == 0) *(u32x2*)(p.ws + WS_EPOCH) = u32x2{tag0, tag1};
    }
    TS(13);
  }
}

extern "C" void kernel_launch(void* const* d_in, const int* in_sizes, int n_in, void* d_out, int out_size, void* d_ws, size_t ws_size,
                              hipStream_t stream) {
  Params p;
  p.x = d_in[0]; p.kc = d_in[1]; p.vc = d_in[2]; p.wq = d_in[3]; p.wo = d_in[4];
  p.w1 = d_in[6]; p.w2 = d_in[7]; p.c = d_in[8]; p.wm = d_in[9]; p.out = d_out; p.ws = (char*)d_ws;
  (void)hipFuncSetAttribute((const void*)fused_block, hipFuncAttributeMaxDynamicSharedMemorySize, L_END);
  hipLaunchKernelGGL(fused_block, dim3(NWG), dim3(NTHR), L_END, stream, p);
}
```

```cpp
#include <hip/hip_runtime.h>
#include <stdint.h>

#define MIMIC 1
#define RING 3
#define WIN0 3
#define WIN 3
#define ABL 0
#define HPRE 1
#define MPF 1
#define DRY 1
#define CODEPF 1

typedef unsigned u32;
using u32x2 = __attribute__((ext_vector_type(2))) unsigned;
using u32x4 = __attribute__((ext_vector_type(4))) unsigned;
using f32x2 = __attribute__((ext_vector_type(2))) float;
using f32x4 = __attribute__((ext_vector_type(4))) float;
using s16x4 = __attribute__((ext_vector_type(4))) short;
using bf16x2 = __attribute__((ext_vector_type(2))) __bf16;
using bf16x8 = __attribute__((ext_vector_type(8))) __bf16;
typedef __amdgpu_buffer_rsrc_t rsrc_t;
#define LAS __attribute__((address_space(3)))
#define DI __device__ __forceinline__

constexpr int NWG = 256, NTHR = 576;
constexpr int DM = 576, NMOD = 3456, NQ = 4096, DFF = 4608;
constexpr int AUX_NT = 2, AUX_SC1 = 16;
constexpr u32 SENT = 0xFFFFFFFFu;
constexpr u32 SPIN_CAP = 1u << 16;

constexpr u32 WS_EPOCH = 0;
constexpr u32 WS_HG = 4096;
constexpr u32 WS_MG = WS_HG + 18432;
constexpr u32 WS_QG = WS_MG + 73728;
constexpr u32 WS_CLR = WS_QG + 131072;
constexpr u32 WS_OP = WS_CLR;
constexpr u32 WS_ML = WS_OP + 131072;
constexpr u32 WS_X2 = WS_ML + 2048;
constexpr u32 WS_FP = WS_CLR + 36 * 6144;
constexpr u32 WS_END = WS_FP + 144 * 18432;
constexpr u32 WS_STAMP = (WS_END + 4095) & ~4095u;
static_assert(WS_X2 + 73728 <= WS_FP, "cleared region");

constexpr u32 L_WM = 0;
constexpr u32 L_WQ = 36864;
constexpr u32 L_WT = 55296;
constexpr u32 L_RED = 129024;
constexpr u32 L_ST = L_RED + 9216;
constexpr u32 L_Q = L_ST + 64;
constexpr u32 L_NX = L_Q + 512;
constexpr u32 L_R = L_NX + 512;
constexpr u32 L_X = L_R + 1152;
constexpr u32 L_EP = L_X + 9216;
constexpr u32 L_TS = L_EP + 256;
constexpr u32 L_END = L_TS;
constexpr u32 L_MOD = 0;
constexpr u32 L_GB = 27648;
constexpr u32 L_XR = 27648 + 64;
constexpr u32 L_AO = 28160;
constexpr u32 L_AM = L_AO + 8192;
constexpr u32 L_H2 = 36864;
constexpr u32 H2S = 1168;

struct Params {
  const void* x; const void* kc; const void* vc; const void* wq; const void* wo;
  const void* w1; const void* w2; const void* c; const void* wm; void* out; char* ws;
};

DI rsrc_t mk(const void* p, u32 n) { return __builtin_amdgcn_make_buffer_rsrc((void*)p, 0, n, 0x00020000); }
template <int AUX> DI u32x4 ld128(rsrc_t r, u32 v, u32 s) { return __builtin_amdgcn_raw_buffer_load_b128(r, v, s, AUX); }
template <int AUX> DI u32x2 ld64(rsrc_t r, u32 v, u32 s) { return __builtin_amdgcn_raw_buffer_load_b64(r, v, s, AUX); }
template <int AUX> DI u32 ld32(rsrc_t r, u32 v, u32 s) { return __builtin_amdgcn_raw_buffer_load_b32(r, v, s, AUX); }
DI void st128(rsrc_t r, u32x4 d, u32 v, u32 s) { __builtin_amdgcn_raw_buffer_store_b128(d, r, v, s, AUX_SC1); }
DI void st32(rsrc_t r, u32 d, u32 v, u32 s) { __builtin_amdgcn_raw_buffer_store_b32(d, r, v, s, AUX_SC1); }
DI void dma(rsrc_t r, u32 v, u32 s, u32 lds) {
  u32 keep;
  asm volatile("s_mov_b32 %0, m0\n\ts_mov_b32 m0, %4\n\ts_nop 3\n\tbuffer_load_dwordx4 %1, %2, %3 offen nt lds\n\ts_mov_b32 m0, %0"
               : "=&s"(keep) : "v"(v), "s"(r), "s"(s), "s"(lds) : "memory");
}
#define SHPOL "nt"
#define ROT 0
#define WFIRST 1
DI void dmas(rsrc_t r, u32 v, u32 s, u32 lds) {
  u32 keep;
  asm volatile("s_mov_b32 %0, m0\n\ts_mov_b32 m0, %4\n\ts_nop 3\n\tbuffer_load_dwordx4 %1, %2, %3 offen " SHPOL " lds\n\ts_mov_b32 m0, %0"
               : "=&s"(keep) : "v"(v), "s"(r), "s"(s), "s"(lds) : "memory");
}
DI void dma4(rsrc_t r, u32 v, u32 lds) {
  u32 keep;
  asm volatile("s_mov_b32 %0, m0\n\ts_mov_b32 m0, %3\n\ts_nop 3\n\tbuffer_load_dword %1, %2, 0 offen nt lds\n\ts_mov_b32 m0, %0"
               : "=&s"(keep) : "v"(v), "s"(r), "s"(lds) : "memory");
}
#define VMCNT(n) asm volatile("s_waitcnt vmcnt(" #n ")" ::: "memory")
template <int N> DI void vmcnt() { asm volatile("s_waitcnt vmcnt(%0)" ::"i"(N) : "memory"); }
DI void bar() { asm volatile("s_waitcnt lgkmcnt(0)\n\ts_barrier" ::: "memory"); }
#define SCHED() __builtin_amdgcn_sched_barrier(0)

DI u32 pk(float lo, float hi) { f32x2 v = {lo, hi}; return __builtin_bit_cast(u32, __builtin_convertvector(v, bf16x2)); }
DI float blo(u32 u) { return __builtin_bit_cast(float, u << 16); }
DI float bhi(u32 u) { return __builtin_bit_cast(float, u & 0xffff0000u); }
DI float rb(float x) { return blo(pk(x, x)); }
DI float asf(u32 u) { return __builtin_bit_cast(float, u); }
DI u32 asu(float f) { return __builtin_bit_cast(u32, f); }
DI u32 nosent(u32 u) { return u == SENT ? 0x7FC07FC0u : u; }
template <int CTRL> DI u32 dppu(u32 x) { return __builtin_amdgcn_mov_dpp(x, CTRL, 0xf, 0xf, true); }
template <int CTRL> DI float dppf(float x) { return asf(dppu<CTRL>(asu(x))); }
constexpr int XOR1 = 0xB1, XOR2 = 0x4E, HMIR = 0x141, ROR8 = 0x128;
DI float hi8(float a, float b) { return asf(__builtin_amdgcn_update_dpp(asu(a), asu(b), ROR8, 0xf, 0xc, false)); }
DI float xg_max(float x) {
  auto s = __builtin_amdgcn_permlane16_swap(asu(x), asu(x), false, false);
  x = fmaxf(asf(s[0]), asf(s[1]));
  auto t = __builtin_amdgcn_permlane32_swap(asu(x), asu(x), false, false);
  return fmaxf(asf(t[0]), asf(t[1]));
}
DI float xg_sum(float x) {
  auto s = __builtin_amdgcn_permlane16_swap(asu(x), asu(x), false, false);
  x = asf(s[0]) + asf(s[1]);
  auto t = __builtin_amdgcn_permlane32_swap(asu(x), asu(x), false, false);
  return asf(t[0]) + asf(t[1]);
}
DI float wave_sum(float x) {
  x += dppf<XOR1>(x); x += dppf<XOR2>(x); x += dppf<HMIR>(x); x += dppf<ROR8>(x);
  return xg_sum(x);
}
DI f32x4 mfma(u32x4 a, u32x4 b, f32x4 c) {
  return __builtin_amdgcn_mfma_f32_16x16x32_bf16(__builtin_bit_cast(bf16x8, a), __builtin_bit_cast(bf16x8, b), c, 0, 0, 0);
}
DI u32x2 trd(LAS char* p) { return __builtin_bit_cast(u32x2, __builtin_amdgcn_ds_read_tr16_b64_v4i16((LAS s16x4*)p)); }
DI u32x4 trfrag(LAS char* p, u32 second) { u32x2 a = trd(p), b = trd(p + second); return u32x4{a[0], a[1], b[0], b[1]}; }
DI float silu(float x) { return x * __builtin_amdgcn_rcpf(1.f + __builtin_amdgcn_exp2f(-1.4426950408889634f * x)); }
DI u32 umax(u32 a, u32 b) { return a > b ? a : b; }
DI u32 mx4(u32 m, u32x4 v) { return umax(umax(umax(m, v[0]), v[1]), umax(v[2], v[3])); }

DI void ln_stats(const float (&v)[12], bool tail, float& mean_b, float& den_b) {
  float s = 0.f;
#pragma unroll
  for (int i = 0; i < 8; ++i) s += v[i];
  float s2 = (v[8] + v[9]) + (v[10] + v[11]);
  s += tail ? s2 : 0.f;
  const float mean = wave_sum(s) * (1.f / DM);
  float q = 0.f, q2 = 0.f;
#pragma unroll
  for (int i = 0; i < 8; ++i) { const float d = v[i] - mean; q = fmaf(d, d, q); }
#pragma unroll
  for (int i = 8; i < 12; ++i) { const float d = v[i] - mean; q2 = fmaf(d, d, q2); }
  q += tail ? q2 : 0.f;
  const float var = wave_sum(q) * (1.f / DM);
  mean_b = rb(mean);
  den_b = rb(__builtin_sqrtf(rb(rb(var) + 1e-5f)));
}
#define TS(i) do {} while (0)

__global__ void __launch_bounds__(NTHR, 3) fused_block(Params p) {
  extern __shared__ __attribute__((aligned(16))) char smem_[];
  LAS char* sm = (LAS char*)smem_;
  const u32 lds0 = (u32)(uintptr_t)sm;
  const int tid = threadIdx.x, lane = tid & 63, w = __builtin_amdgcn_readfirstlane(tid >> 6);
  const int bid = blockIdx.x;
  {
    uint64_t pc; u32 len;
    asm volatile("s_getpc_b64 %0\n.Lfb_code_begin:\n\ts_mov_b32 %1, .Lfb_code_end-.Lfb_code_begin" : "=s"(pc), "=s"(len));
    const rsrc_t rcode = mk((const void*)pc, len);
    u32 keep;
    asm volatile("s_mov_b32 %0, m0\n\ts_mov_b32 m0, %3\n\ts_nop 3\n\tbuffer_load_dword %1, %2, 0 offen lds\n\ts_mov_b32 m0, %0"
                 : "=&s"(keep) : "v"(tid * 64), "s"(rcode), "s"(lds0 + L_R) : "memory");
  }
  TS(0);
  asm volatile("" : "+s"(p.x), "+s"(p.kc), "+s"(p.vc), "+s"(p.wq), "+s"(p.wo), "+s"(p.w1), "+s"(p.w2), "+s"(p.c), "+s"(p.wm), "+s"(p.out), "+s"(p.ws));
  const int g = lane >> 4, n = lane & 15;
  const int G = (bid >> 5) * 8 + (bid & 7), r4 = (bid >> 3) & 3;
  const bool roleM = G < 9, roleO = G >= 10 && G < 28, roleF = G >= 28;
  const int md0 = G * 64 + r4 * 16;
  const int oi = (G - 10) * 4 + r4, oks = oi / 18, oct = oi - oks * 18;
  const int fj = (G - 28) * 4 + r4;
  const int qt = G * 4 + r4;
  const rsrc_t rws = mk(p.ws, WS_END), rx = mk(p.x, 8 * DM * 2);
  const u32 frow = ((lane >> 3) & 3) * 8 + (lane >> 5) * 4 + ((lane >> 1) & 3);
  const bool tail = lane < 16;
  const u32 q3 = tail ? lane * 8 + 1024 : 0;

  const rsrc_t rm = mk(p.wm, DM * NMOD * 2);
  if (w == 8) {
    __builtin_amdgcn_s_setprio(3);
    dma4(rws, WS_EPOCH + (lane & 1) * 4, lds0 + L_EP);
  }
  if (!roleM && w < 2) {
    const int x = bid & 7, u = (bid >> 3) * 42 - 168 + lane;
    if (bid >= 32 && (w == 0 || x == 0)) {
      const u32 v = (lane < 42 && u < 1152) ? (u >> 1) * (NMOD * 2) + (u & 1) * 1152 + (w ? 8 : x) * 128 : 0x7fffffffu;
      dma4(rm, v, lds0 + L_R);
    }
  }
  if (roleM) {
    const rsrc_t rc = mk(p.c, 8 * DM * 2);
    const u32 mv = frow * (NMOD * 2) + (lane & 1) * 16;
    int wr = w + 2 * r4; wr = wr >= 9 ? wr - 9 : wr;
#pragma unroll
    for (int i = 0; i < 2; ++i) {
      const int s = 2 * wr + i;
      dma(rm, mv, s * 32 * NMOD * 2 + md0 * 2, lds0 + L_WM + s * 1024);
      dma(rm, mv, s * 32 * NMOD * 2 + (576 + md0) * 2, lds0 + L_WM + 18432 + s * 1024);
    }
    u32x2 cv[2], xv[3];
#pragma unroll
    for (int i = 0; i < 2; ++i) cv[i] = ld64<0>(rc, (n & 7) * 1152 + g * 16 + (n >> 3) * 8, (2 * wr + i) * 64);
#pragma unroll
    for (int i = 0; i < 3; ++i) xv[i] = ld64<0>(rx, i < 2 ? lane * 8 + i * 512 : q3, (w & 7) * 1152);
    if (w < 6) st128(rws, u32x4{SENT, SENT, SENT, SENT}, lane * 16, WS_CLR + qt * 6144 + w * 1024);
    SCHED();
    u32x4 bf[2];
#pragma unroll
    for (int i = 0; i < 2; ++i) {
      const u32 a = pk(silu(blo(cv[i][0])), silu(bhi(cv[i][0]))), b = pk(silu(blo(cv[i][1])), silu(bhi(cv[i][1])));
      bf[i] = u32x4{a, b, dppu<ROR8>(a), dppu<ROR8>(b)};
    }
    TS(1);
    f32x4 dA = {0.f, 0.f, 0.f, 0.f}, dB = dA;
#pragma unroll
    for (int i = 0; i < 2; ++i) {
      asm volatile("" : "+v"(bf[i]) :: "memory");
      LAS char* a = sm + L_WM + (2 * wr + i) * 1024 + lane * 8;
      dA = mfma(trfrag(a, 512), bf[i], dA);
      dB = mfma(trfrag(a + 18432, 512), bf[i], dB);
      if (n < 8) *(LAS u32x4*)(sm + L_X + (2 * wr + i) * 512 + (g * 8 + n) * 16) = bf[i];
    }
    *(LAS f32x4*)(sm + L_RED + wr * 1024 + lane * 16) = f32x4{hi8(dA[0], dB[0]), hi8(dA[1], dB[1]), hi8(dA[2], dB[2]), hi8(dA[3], dB[3])};
    if (w < 8) {
      float v[12];
#pragma unroll
      for (int i = 0; i < 3; ++i) { v[4 * i] = blo(xv[i][0]); v[4 * i + 1] = bhi(xv[i][0]); v[4 * i + 2] = blo(xv[i][1]); v[4 * i + 3] = bhi(xv[i][1]); }
      float mb, db;
      ln_stats(v, tail, mb, db);
      const int q0 = md0 >> 2, qi = q0 >> 6;
      f32x4 o;
#pragma unroll
      for (int k = 0; k < 4; ++k) o[k] = rb(rb((qi == 0 ? v[k] : qi == 1 ? v[4 + k] : v[8 + k]) - mb) / db);
      const int ql = lane - (q0 & 63);
      if (ql >= 0 && ql < 4) *(LAS f32x4*)(sm + L_NX + w * 64 + ql * 16) = o;
      VMCNT(0);
    }
  }

  if (w == 8) {
    {
      const rsrc_t rq = mk(p.wq, DM * NQ * 2);
      const u32 qv = frow * (NQ * 2) + (lane & 1) * 16;
      int s = r4 * 4;
#pragma unroll 1
      for (int i = 0; i < 18; ++i) { dma(rq, qv, s * 32 * NQ * 2 + qt * 32, lds0 + L_WQ + s * 1024); s = s == 17 ? 0 : s + 1; }
    }
    VMCNT(18);
    const u32x2 ep = *(LAS u32x2*)(sm + L_EP);
    const u32 tag0 = __builtin_amdgcn_readfirstlane(ep[0]) + 1u, tag1 = __builtin_amdgcn_readfirstlane(ep[1]) + 1u;
    if (roleM) {
      bar();
      f32x4 s = *(LAS f32x4*)(sm + L_RED + lane * 16);
#pragma unroll
      for (int i = 1; i < 9; ++i) s += *(LAS f32x4*)(sm + L_RED + i * 1024 + lane * 16);
      const u32 a = pk(s[0], s[1]), b = pk(s[2], s[3]);
      const u32 ca = dppu<ROR8>(a), cb = dppu<ROR8>(b);
      const f32x4 nx = *(LAS f32x4*)(sm + L_NX + (n & 7) * 64 + g * 16);
      const u32 c0 = pk(1.f + blo(ca), 1.f + bhi(ca)), c1 = pk(1.f + blo(cb), 1.f + bhi(cb));
      const u32 m0 = pk(nx[0] * blo(c0), nx[1] * bhi(c0)), m1 = pk(nx[2] * blo(c1), nx[3] * bhi(c1));
      const u32 h0 = pk(blo(m0) + blo(a), bhi(m0) + bhi(a)), h1 = pk(blo(m1) + blo(b), bhi(m1) + bhi(b));
      if (n < 8) st128(rws, u32x4{h0, tag0, h1, tag1}, (g * 8 + n) * 16, WS_HG + md0 * 32);
      TS(2);
    }
    VMCNT(0);
    u32x4 hg[18];
    for (u32 spin = 0;; ++spin) {
      asm volatile("" ::: "memory");
      const u32 t = ld32<AUX_SC1>(rws, 4, WS_HG + (bid % 36) * 512);
      if (__all(t == tag0) || spin > SPIN_CAP) break;
    }
    for (u32 spin = 0;; ++spin) {
      asm volatile("" ::: "memory");
#pragma unroll
      for (int s = 0; s < 18; ++s) hg[s] = ld128<AUX_SC1>(rws, lane * 16 + s * 1024, WS_HG);
      u32 bad = 0;
#pragma unroll
      for (int s = 0; s < 18; ++s) bad |= (hg[s][1] ^ tag0) | (hg[s][3] ^ tag1);
      if (__all(bad == 0) || spin > SPIN_CAP) break;
    }
    TS(3);
    {
      f32x4 d0 = {0.f, 0.f, 0.f, 0.f}, d1 = d0;
#pragma unroll
      for (int s = 0; s < 18; s += 2) {
        d0 = mfma(trfrag(sm + L_WQ + s * 1024 + lane * 8, 512), u32x4{hg[s][0], hg[s][2], dppu<ROR8>(hg[s][0]), dppu<ROR8>(hg[s][2])}, d0);
        d1 = mfma(trfrag(sm + L_WQ + (s + 1) * 1024 + lane * 8, 512), u32x4{hg[s + 1][0], hg[s + 1][2], dppu<ROR8>(hg[s + 1][0]), dppu<ROR8>(hg[s + 1][2])}, d1);
      }
      d0 += d1;
      if (n < 8) st128(rws, u32x4{pk(d0[0], d0[1]), tag0, pk(d0[2], d0[3]), tag1}, n * 16384 + g * 16, WS_QG + qt * 64);
    }
    TS(4);
    {
      u32x4 qg;
      for (u32 spin = 0;; ++spin) {
        asm volatile("" ::: "memory");
        qg = ld128<AUX_SC1>(rws, lane * 16, WS_QG + (bid >> 1) * 1024);
        if (__all((qg[1] == tag0) & (qg[3] == tag1)) || spin > SPIN_CAP) break;
      }
      *(LAS u32x2*)(sm + L_Q + lane * 8) = u32x2{qg[0], qg[2]};
    }
    TS(5);
    bar();
    if (roleM && !(ABL & 1)) {
      const u32 mv = (lane >> 3) * (NMOD * 2) + (lane & 7) * 16;
#pragma unroll 1
      for (int i = 0; i < 72; ++i) { dma(rm, mv, i * 8 * NMOD * 2 + (1152 + qt * 64) * 2, lds0 + L_WT + i * 1024); VMCNT(11); }
      VMCNT(0);
      f32x4 d[4];
#pragma unroll
      for (int t = 0; t < 4; ++t) d[t] = f32x4{0.f, 0.f, 0.f, 0.f};
#pragma unroll 1
      for (int s = 0; s < 18; ++s) {
        const u32x4 b = *(LAS u32x4*)(sm + L_X + s * 512 + (g * 8 + (n & 7)) * 16);
#pragma unroll
        for (int t = 0; t < 4; ++t) d[t] = mfma(trfrag(sm + L_WT + s * 4096 + (g * 8 + ((lane >> 2) & 3)) * 128 + t * 32 + (lane & 3) * 8, 512), b, d[t]);
      }
      const int k9 = qt / 9, kind = k9 == 0 ? 2 : k9 == 3 ? 3 : k9 - 1;
#pragma unroll
      for (int t = 0; t < 2; ++t) {
        const f32x4 e = n < 8 ? d[2 * t] : d[2 * t + 1];
        st128(rws, u32x4{pk(e[0], e[1]), tag0, pk(e[2], e[3]), tag1}, (n & 7) * 2304 + (t * 8 + (n >> 3) * 4 + g) * 16, WS_MG + kind * 18432 + (qt - k9 * 9) * 256);
      }
    }
    if (roleM) TS(8);
    if (roleF && !(ABL & 1)) {
#pragma unroll 1
      for (int i = 0; i < ((ABL & 8) ? 0 : 18); ++i) st128(rws, u32x4{SENT, SENT, SENT, SENT}, lane * 16, WS_FP + fj * 18432 + i * 1024);
#pragma unroll 1
      for (int i = 0; i < 9; ++i) dma(rx, lane * 16, i * 1024, lds0 + L_X + i * 1024);
      for (u32 spin = 0;; ++spin) {
        __builtin_amdgcn_s_sleep(127);
        asm volatile("" ::: "memory");
        const int u = lane < 36 ? lane : 0, k9 = u / 9;
        const u32 t = ld32<AUX_SC1>(rws, k9 * 18432 + (u - k9 * 9) * 256 + 4, WS_MG);
        if (__all(t == tag0) || spin > SPIN_CAP) break;
      }
      TS(9);
#pragma unroll 1
      for (int c3 = 0; c3 < 3; ++c3) {
        for (u32 spin = 0;; ++spin) {
          asm volatile("" ::: "memory");
#pragma unroll
          for (int i = 0; i < 18; ++i) hg[i] = ld128<AUX_SC1>(rws, lane * 16 + i * 1024, WS_MG + c3 * 18432);
          u32 bad = 0;
#pragma unroll
          for (int i = 0; i < 18; ++i) bad |= (hg[i][1] ^ tag0) | (hg[i][3] ^ tag1);
          if (__all(bad == 0) || spin > SPIN_CAP) break;
          __builtin_amdgcn_s_sleep(64);
        }
#pragma unroll
        for (int i = 0; i < 18; ++i) *(LAS u32x2*)(sm + L_MOD + (c3 * 18 + i) * 512 + lane * 8) = u32x2{hg[i][0], hg[i][2]};
      }
      u32x4 m;
      for (u32 spin = 0;; ++spin) {
        asm volatile("" ::: "memory");
        m = ld128<AUX_SC1>(rws, (lane & 7) * 16, WS_MG + 3 * 18432 + fj * 128);
        if (__all((m[1] == tag0) & (m[3] == tag1)) || spin > SPIN_CAP) break;
      }
      if (lane < 8) *(LAS u32x2*)(sm + L_GB + lane * 8) = u32x2{m[0], m[2]};
      if (lane == 0) *(LAS u32x2*)(sm + L_ST) = u32x2{tag0, tag1};
    }
    TS(6);
  } else {
    const rsrc_t rk = mk((const char*)p.kc + (size_t)bid * 524288, 524288);
    const rsrc_t rv = mk((const char*)p.vc + (size_t)bid * 524288, 524288);
    const u32 koff = (n & 7) * 256 + (n >> 3) * 64 + g * 16, voff = g * 1024 + n * 16;
    u32x4 KB[RING][4], VB[RING][4];
#define LOADK(slot, t, W) _Pragma("unroll") for (int i_ = 0; i_ < 4; ++i_) { vmcnt<(W) - 1>(); SCHED(); \
      KB[slot][i_] = ld128<AUX_NT>(rk, koff + (i_ & 1) * 128 + (i_ >> 1) * 2048, ((t) * 8 + w) * 4096); SCHED(); }
#define LOADV(slot, t, W) _Pragma("unroll") for (int i_ = 0; i_ < 4; ++i_) { vmcnt<(W) - 1>(); SCHED(); \
      VB[slot][i_] = ld128<AUX_NT>(rv, voff + i_ * 256, ((t) * 8 + w) * 4096); SCHED(); }
    if (roleM) bar();
    if ((roleO | roleF) && !(ABL & 2)) {
      const rsrc_t r1 = roleO ? mk(p.wo, NQ * DM * 2) : mk(p.w1, DM * DFF * 2);
      const u32 rs = roleO ? DM * 2 : DFF * 2;
      const u32 v1 = (g * 8 + ((lane >> 2) & 3)) * rs + (lane & 3) * 16;
      const u32 base = roleO ? oks * 1024 * DM * 2 + oct * 64 : fj * 64;
      const int nu = roleO ? 64 : 36;
      int u = w + ROT * (r4 & 1) * (roleO ? 32 : 16);
#pragma unroll 1
      for (int i = w; i < nu; i += 8) {
        vmcnt<WIN0 - 1>();
        dmas(r1, v1, ((u >> 1) * 32 + (u & 1) * 4) * rs + base, lds0 + L_WT + u * 1024);
        u += 8; u = u >= nu ? w : u;
      }
      if (roleF) {
        const rsrc_t r2 = mk(p.w2, DFF * DM * 2);
#pragma unroll 1
        for (int i = 7 - w; i < 36; i += 8) { vmcnt<WIN0 - 1>(); dma(r2, lane * 16, fj * 36864 + i * 1024, lds0 + L_WT + 36864 + i * 1024); }
      }
    }
#pragma unroll
    for (int t = 0; t < RING; ++t) { LOADK(t, t, WIN0); LOADV(t, t, WIN0); }
    TS(2);
    bar();
    TS(5);
    u32x4 qf[2];
#pragma unroll
    for (int i = 0; i < 2; ++i) qf[i] = *(LAS u32x4*)(sm + L_Q + (n >> 3) * 256 + i * 128 + (n & 1) * 64 + g * 16);
    u32 pm[4];
#pragma unroll
    for (int i = 0; i < 4; ++i) pm[i] = ((n & 7) >> 1) == i ? ((n & 1) ? 0xffff0000u : 0x0000ffffu) : 0u;
    float am = -INFINITY, al = 0.f, e[4];
    f32x4 acc = {0.f, 0.f, 0.f, 0.f}, s;
    constexpr float SC = 0.08838834764831845f, L2E = 1.4426950408889634f;
#define QK(sl) do { const f32x4 z_ = {0.f, 0.f, 0.f, 0.f}; \
      const f32x4 d1_ = mfma(KB[sl][1], qf[1], mfma(KB[sl][0], qf[0], z_)), d2_ = mfma(KB[sl][3], qf[1], mfma(KB[sl][2], qf[0], z_)); \
      _Pragma("unroll") for (int i_ = 0; i_ < 4; ++i_) { \
        auto x_ = __builtin_amdgcn_permlane32_swap(asu(d1_[i_]), asu(d2_[i_]), false, false); \
        s[i_] = dppf<0xA0>(asf(x_[0])) + dppf<0xF5>(asf(x_[1])); } } while (0)
#define SCORES() do { const u32 a_ = pk(s[0], s[1]), b_ = pk(s[2], s[3]); \
      const u32 a2_ = pk(blo(a_) * SC, bhi(a_) * SC), b2_ = pk(blo(b_) * SC, bhi(b_) * SC); \
      e[0] = blo(a2_); e[1] = bhi(a2_); e[2] = blo(b2_); e[3] = bhi(b2_); } while (0)
#define SOFTMAX() do { SCORES(); \
      const float mn_ = fmaxf(am, xg_max(fmaxf(fmaxf(e[0], e[1]), fmaxf(e[2], e[3])))); \
      const float alpha_ = __builtin_amdgcn_exp2f((am - mn_) * L2E); am = mn_; \
      _Pragma("unroll") for (int i_ = 0; i_ < 4; ++i_) e[i_] = __builtin_amdgcn_exp2f((e[i_] - mn_) * L2E); \
      al = fmaf(al, alpha_, (e[0] + e[1]) + (e[2] + e[3])); acc *= alpha_; } while (0)
#define PV(sl) do { _Pragma("unroll") for (int i_ = 0; i_ < 4; ++i_) { const u32 pb_ = pk(e[i_], e[i_]); \
      acc = mfma(VB[sl][i_], u32x4{pb_ & pm[0], pb_ & pm[1], pb_ & pm[2], pb_ & pm[3]}, acc); } } while (0)
    QK(0); SCHED(); LOADK(0, RING, 64); SOFTMAX(); PV(0); SCHED();
#pragma unroll
    for (int t = 1; t < RING; ++t) { QK(t); SOFTMAX(); PV(t); SCHED(); }
    LOADV(0, RING, WIN);
#define STEP(cur, nxt, t) do { LOADK(nxt, (t) + 1, WIN); QK(cur); SOFTMAX(); SCHED(); LOADV(nxt, (t) + 1, WIN); PV(cur); SCHED(); } while (0)
    static_assert((16 - RING) % 2 == 1, "pairs plus one");
#pragma unroll 1
    for (int t = RING; t < 15; t += 2) { STEP(0, 1, t); STEP(1, 0, t + 1); }
    QK(0); SOFTMAX(); PV(0);
    al = xg_sum(al);
#pragma unroll
    for (int i = 0; i < 4; ++i) *(LAS float*)(sm + L_AO + w * 1024 + ((n >> 3) * 128 + g * 32 + i * 8 + (n & 7)) * 4) = acc[i];
    if (g == 0 && (n & 7) == 0) *(LAS f32x2*)(sm + L_AM + (n >> 3) * 64 + w * 8) = f32x2{am, al};
    TS(6);
  }

  int pass = (DRY && w == 8) ? 0 : 1;
#pragma unroll 1
  do {
    if (ABL) break;
    asm volatile("" : "+v"(pass));
    pass = __builtin_amdgcn_readfirstlane(pass);
    const bool wet = pass != 0;
    const int we = wet ? w : 0;
    const u32 cap = wet ? SPIN_CAP : 0u;
    const rsrc_t rst = mk(p.ws, wet ? WS_END : 0u);
    if (wet) { VMCNT(0); bar(); }
    if (we < 2) {
      constexpr float L2E = 1.4426950408889634f;
      f32x4 ml[4];
#pragma unroll
      for (int i = 0; i < 4; ++i) ml[i] = *(LAS f32x4*)(sm + L_AM + we * 64 + i * 16);
      f32x2 o[8];
#pragma unroll
      for (int i = 0; i < 8; ++i) o[i] = *(LAS f32x2*)(sm + L_AO + i * 1024 + we * 512 + lane * 8);
      float M = ml[0][0];
#pragma unroll
      for (int i = 1; i < 8; ++i) M = fmaxf(M, ml[i >> 1][(i & 1) * 2]);
      float L = 0.f, o0 = 0.f, o1 = 0.f;
#pragma unroll
      for (int i = 0; i < 8; ++i) {
        const float a = __builtin_amdgcn_exp2f((ml[i >> 1][(i & 1) * 2] - M) * L2E);
        L = fmaf(a, ml[i >> 1][(i & 1) * 2 + 1], L); o0 = fmaf(a, o[i][0], o0); o1 = fmaf(a, o[i][1], o1);
      }
      const float inv = __builtin_amdgcn_rcpf(L);
      const int slot = (((bid >> 1) & 15) * 2 + we) * 16 + (bid & 1) * 8 + (bid >> 5);
      st32(rst, nosent(pk(o0 * inv, o1 * inv)), lane * 4, WS_OP + slot * 256);
      if (lane == 0) st32(rst, nosent(asu(fmaf(M, L2E, __builtin_amdgcn_logf(L)))), 0, WS_ML + slot * 4);
    }
    if (wet) TS(7);

    if (roleO) {
      if (we < 8) {
        u32x4 wf[8], ob[4]; u32 ls;
#pragma unroll
        for (int i = 0; i < 8; ++i) wf[i] = trfrag(sm + L_WT + (we * 4 + (i >> 1)) * 2048 + (lane >> 2) * 64 + (i & 1) * 32 + (lane & 3) * 8, 1024);
        const int h0 = oks * 8 + we;
        for (u32 spin = 0;; ++spin) {
          asm volatile("" ::: "memory");
          ls = ld32<AUX_SC1>(rws, n * 4, WS_ML + h0 * 64);
#pragma unroll
          for (int i = 0; i < 4; ++i) ob[i] = ld128<AUX_SC1>(rws, n * 256 + g * 16 + i * 64, WS_OP + h0 * 4096);
          u32 m = ls;
#pragma unroll
          for (int i = 0; i < 4; ++i) m = mx4(m, ob[i]);
          if (!__any(m == SENT) || spin >= cap) break;
        }
        if (wet) TS(8);
        f32x4 d0 = {0.f, 0.f, 0.f, 0.f}, d1 = d0;
#pragma unroll
        for (int i = 0; i < 4; ++i) { d0 = mfma(wf[2 * i], ob[i], d0); d1 = mfma(wf[2 * i + 1], ob[i], d1); }
        const float a0 = __builtin_amdgcn_rcpf(1.f + __builtin_amdgcn_exp2f(dppf<ROR8>(asf(ls)) - asf(ls)));
        d0 *= a0; d1 *= a0;
        f32x4 e;
#pragma unroll
        for (int i = 0; i < 4; ++i) e[i] = (n < 8 ? d0[i] : d1[i]) + dppf<ROR8>(n < 8 ? d1[i] : d0[i]);
        *(LAS f32x4*)(sm + L_RED + we * 1024 + lane * 16) = e;
      }
      if (wet) bar();
      if (we == 0) {
        f32x4 s = *(LAS f32x4*)(sm + L_RED + lane * 16);
#pragma unroll
        for (int i = 1; i < 8; ++i) s += *(LAS f32x4*)(sm + L_RED + i * 1024 + lane * 16);
        st128(rst, u32x4{nosent(asu(s[0])), nosent(asu(s[1])), nosent(asu(s[2])), nosent(asu(s[3]))}, (n & 7) * 2304 + (n >> 3) * 64 + g * 16, WS_X2 + oks * 18432 + oct * 128);
        if (wet) TS(9);
      }
    }

    if (roleF) {
      const int jb = fj / 18, jc = fj - jb * 18;
      u32x4 w1f[4], w2f[4];
#pragma unroll
      for (int i = 0; i < 4; ++i) {
        w1f[i] = trfrag(sm + L_WT + (2 * we + (i >> 1)) * 2048 + (lane >> 2) * 64 + (i & 1) * 32 + (lane & 3) * 8, 1024);
        w2f[i] = trfrag(sm + L_WT + 36864 + (lane >> 2) * 1152 + (4 * we + i) * 32 + (lane & 3) * 8, 16 * 1152);
      }
      if (we < 8) {
        f32x4 xv[3], sb[3], cb[3], ga[3];
#pragma unroll
        for (int i = 0; i < 3; ++i) {
          const u32 o = we * 1152 + (i < 2 ? lane * 8 + i * 512 : q3);
          const u32x2 a = *(LAS u32x2*)(sm + L_X + o), b = *(LAS u32x2*)(sm + L_MOD + o), c = *(LAS u32x2*)(sm + L_MOD + 9216 + o), d = *(LAS u32x2*)(sm + L_MOD + 18432 + o);
          xv[i] = f32x4{blo(a[0]), bhi(a[0]), blo(a[1]), bhi(a[1])};
          sb[i] = f32x4{blo(b[0]), bhi(b[0]), blo(b[1]), bhi(b[1])};
          cb[i] = f32x4{rb(1.f + blo(c[0])), rb(1.f + bhi(c[0])), rb(1.f + blo(c[1])), rb(1.f + bhi(c[1]))};
          ga[i] = f32x4{blo(d[0]), bhi(d[0]), blo(d[1]), bhi(d[1])};
          asm volatile("" : "+v"(xv[i]), "+v"(sb[i]), "+v"(cb[i]), "+v"(ga[i]));
        }
        u32x4 pa[12];
        for (u32 spin = 0;; ++spin) {
          asm volatile("" ::: "memory");
#pragma unroll
          for (int i = 0; i < 12; ++i) pa[i] = ld128<AUX_SC1>(rws, (i >> 2) < 2 ? lane * 16 + (i >> 2) * 1024 : q3 * 2, WS_X2 + (i & 3) * 18432 + we * 2304);
          u32 m = 0;
#pragma unroll
          for (int i = 0; i < 12; ++i) m = mx4(m, pa[i]);
          if (!__any(m == SENT) || spin >= cap) break;
        }
        if (wet) TS(10);
        f32x4 v[3];
#pragma unroll
        for (int i = 0; i < 3; ++i) v[i] = xv[i] + ga[i] * ((__builtin_bit_cast(f32x4, pa[4 * i]) + __builtin_bit_cast(f32x4, pa[4 * i + 1])) +
                                                           (__builtin_bit_cast(f32x4, pa[4 * i + 2]) + __builtin_bit_cast(f32x4, pa[4 * i + 3])));
        const float piv = asf(__builtin_amdgcn_readfirstlane(asu(v[0][0])));
        const f32x4 d0 = v[0] - piv, d1 = v[1] - piv, d2 = tail ? v[2] - piv : f32x4{0.f, 0.f, 0.f, 0.f};
        const f32x4 s1 = d0 + d1 + d2, s2 = d0 * d0 + d1 * d1 + d2 * d2;
        const float md = wave_sum((s1[0] + s1[1]) + (s1[2] + s1[3])) * (1.f / DM), sq = wave_sum((s2[0] + s2[1]) + (s2[2] + s2[3])) * (1.f / DM);
        const float rs = __builtin_amdgcn_rsqf(fmaxf(sq - md * md, 0.f) + 1e-5f);
        const f32x4 h0 = (d0 - md) * rs * cb[0] + sb[0], h1 = (d1 - md) * rs * cb[1] + sb[1], h2 = (d2 - md) * rs * cb[2] + sb[2];
        *(LAS u32x2*)(sm + L_H2 + we * H2S + lane * 8) = u32x2{pk(h0[0], h0[1]), pk(h0[2], h0[3])};
        *(LAS u32x2*)(sm + L_H2 + we * H2S + lane * 8 + 512) = u32x2{pk(h1[0], h1[1]), pk(h1[2], h1[3])};
        if (tail) *(LAS u32x2*)(sm + L_H2 + we * H2S + lane * 8 + 1024) = u32x2{pk(h2[0], h2[1]), pk(h2[2], h2[3])};
        if (we == jb || !wet) {
          const int qi = jc >> 3, ql = lane - (jc & 7) * 8;
          if (ql >= 0 && ql < 8) *(LAS f32x4*)(sm + L_XR + ql * 16) = qi == 0 ? v[0] : qi == 1 ? v[1] : v[2];
        }
      }
      if (wet) bar();
      {
        f32x4 d0 = {0.f, 0.f, 0.f, 0.f}, d1 = d0;
#pragma unroll
        for (int i = 0; i < 2; ++i) {
          const u32x4 hb = *(LAS u32x4*)(sm + L_H2 + (n & 7) * H2S + (2 * we + i) * 64 + g * 16);
          d0 = mfma(w1f[2 * i], hb, d0); d1 = mfma(w1f[2 * i + 1], hb, d1);
        }
        *(LAS f32x4*)(sm + L_RED + we * 1024 + lane * 16) = n < 8 ? d0 : d1;
      }
      if (wet) bar();
      {
        f32x4 s = *(LAS f32x4*)(sm + L_RED + lane * 16);
#pragma unroll
        for (int i = 1; i < 9; ++i) s += *(LAS f32x4*)(sm + L_RED + i * 1024 + lane * 16);
        const u32 a = pk(s[0] < 0.f ? 0.f : s[0], s[1] < 0.f ? 0.f : s[1]), b = pk(s[2] < 0.f ? 0.f : s[2], s[3] < 0.f ? 0.f : s[3]);
        const u32 a2 = dppu<ROR8>(a), b2 = dppu<ROR8>(b);
        const u32x4 hb = n < 8 ? u32x4{a, b, a2, b2} : u32x4{a2, b2, a, b};
        const f32x4 z = {0.f, 0.f, 0.f, 0.f};
#pragma unroll
        for (int i = 0; i < 2; ++i) {
          const f32x4 e0 = mfma(w2f[2 * i], hb, z), e1 = mfma(w2f[2 * i + 1], hb, z);
          const f32x4 e = n < 8 ? e0 : e1;
          st128(rst, u32x4{nosent(asu(e[0])), nosent(asu(e[1])), nosent(asu(e[2])), nosent(asu(e[3]))},
                (n & 7) * (18 * 18432) + (n >> 3) * 64 + g * 16, WS_FP + (2 * we + i) * 18432 + fj * 128);
        }
      }
      if (wet) TS(11);
      u32x4 fa[2];
      for (u32 spin = 0;; ++spin) {
        asm volatile("" ::: "memory");
#pragma unroll
        for (int i = 0; i < 2; ++i) fa[i] = ld128<AUX_SC1>(rws, lane * 16, WS_FP + fj * 18432 + (2 * we + i) * 1024);
        if (!__any(mx4(mx4(0u, fa[0]), fa[1]) == SENT) || spin >= cap) break;
      }
      if (wet) TS(12);
      f32x4 s;
#pragma unroll
      for (int i = 0; i < 4; ++i) { float t = asf(fa[0][i]) + asf(fa[1][i]); t += dppf<ROR8>(t); s[i] = xg_sum(t); }
      if (lane < 8) *(LAS f32x4*)(sm + L_R + we * 128 + lane * 16) = s;
      if (wet) bar();
      if (we == 0 && tail) {
        f32x2 t = *(LAS f32x2*)(sm + L_R + lane * 8);
#pragma unroll
        for (int i = 1; i < 9; ++i) t += *(LAS f32x2*)(sm + L_R + i * 128 + lane * 8);
        const u32 gb = *(LAS u32*)(sm + L_GB + lane * 4);
        const f32x2 xr = *(LAS f32x2*)(sm + L_XR + lane * 8);
        const rsrc_t ro = mk(p.out, wet ? 8 * DM * 2 : 0);
        __builtin_amdgcn_raw_buffer_store_b32(pk(fmaf(blo(gb), t[0], xr[0]), fmaf(bhi(gb), t[1], xr[1])), ro, lane * 4, fj * 64, 0);
        if (fj == 0 && lane < 2) st32(rst, *(LAS u32*)(sm + L_ST + lane * 4), lane * 4, WS_EPOCH);
      }
      if (wet) TS(13);
    }
    if (!wet) TS(14);
  } while (++pass < 2);
  asm volatile(".Lfb_code_end:");
}

extern "C" void kernel_launch(void* const* d_in, const int* in_sizes, int n_in, void* d_out, int out_size, void* d_ws, size_t ws_size,
                              hipStream_t stream) {
  Params p;
  p.x = d_in[0]; p.kc = d_in[1]; p.vc = d_in[2]; p.wq = d_in[3]; p.wo = d_in[4];
  p.w1 = d_in[6]; p.w2 = d_in[7]; p.c = d_in[8]; p.wm = d_in[9]; p.out = d_out; p.ws = (char*)d_ws;
  (void)hipFuncSetAttribute((const void*)fused_block, hipFuncAttributeMaxDynamicSharedMemorySize, L_END);
  hipLaunchKernelGGL(fused_block, dim3(NWG), dim3(NTHR), L_END, stream, p);
}
```

```cpp
#include <hip/hip_runtime.h>
#include <stdint.h>

constexpr int PA = 1320, PB = 222;
constexpr int RING = 3;
constexpr int WIN0 = 4, WINL = 3;
constexpr int WIN = 3, SWIN = 6;
constexpr int PFAR = 220;
constexpr int CK1 = 6, CT1 = 90, CK2 = 9, CT2 = 60;
constexpr int ODL = -45, CPF = 300;
constexpr int W2E = 32;

typedef unsigned u32;
using u32x2 = __attribute__((ext_vector_type(2))) unsigned;
using u32x4 = __attribute__((ext_vector_type(4))) unsigned;
using f32x2 = __attribute__((ext_vector_type(2))) float;
using f32x4 = __attribute__((ext_vector_type(4))) float;
using s16x2 = __attribute__((ext_vector_type(2))) short;
using s16x4 = __attribute__((ext_vector_type(4))) short;
using bf16x2 = __attribute__((ext_vector_type(2))) __bf16;
using bf16x8 = __attribute__((ext_vector_type(8))) __bf16;
typedef __amdgpu_buffer_rsrc_t rsrc_t;
#define LAS __attribute__((address_space(3)))
#define DI __device__ __forceinline__

constexpr int NWG = 256, NTHR = 576;
constexpr int DM = 576, NMOD = 3456, NQ = 4096, DFF = 4608;
constexpr int AUX_NT = 2, AUX_SC1 = 16;
constexpr u32 SENT = 0xFFFFFFFFu;
constexpr u32 SPIN_CAP = 1u << 16;

constexpr u32 WS_EPOCH = 0;
constexpr u32 WS_T0 = 2048;
constexpr u32 WS_HG = 4096;
constexpr u32 WS_MG = WS_HG + 18432;
constexpr u32 WS_QG = WS_MG + 73728;
constexpr u32 WS_ML = WS_QG + 131072;
constexpr u32 WS_CLR = WS_ML + 32768;
constexpr u32 WS_OP = WS_CLR;
constexpr u32 WS_X2 = WS_OP + 131072;
constexpr u32 WS_FP = WS_CLR + 36 * 5120;
constexpr u32 WS_END = WS_FP + 72 * 9216;
static_assert(WS_X2 + 40960 <= WS_FP, "cleared region");

constexpr u32 L_W = 0;
constexpr u32 L_W2 = 73728;
constexpr u32 L_RED = 73728;
constexpr u32 L_SC = L_RED + 9216;
constexpr u32 L_A0 = L_RED + 9216;
constexpr u32 L_OT = L_A0 + 512;
constexpr u32 L_NX = L_SC + 9216;
constexpr u32 L_AO = 147456;
constexpr u32 L_AM = L_AO + 8192;
constexpr u32 L_H2 = L_AO;
constexpr u32 H2S = 1168;
constexpr u32 L_H1 = L_AO + 9344;
constexpr u32 L_XR = L_H1 + 4096;
constexpr u32 L_Q = L_XR + 256;
constexpr u32 L_EP = L_Q + 512;
constexpr u32 L_JUNK = L_EP + 256;
constexpr u32 L_END = L_JUNK + 256;
static_assert(L_END <= 163840, "LDS");

struct Params {
  const void* x; const void* kc; const void* vc; const void* wq; const void* wo;
  const void* w1; const void* w2; const void* c; const void* wm; void* out; char* ws;
};

DI rsrc_t mk(const void* p, u32 n) { return __builtin_amdgcn_make_buffer_rsrc((void*)p, 0, n, 0x00020000); }
template <int AUX> DI u32x4 ld128(rsrc_t r, u32 v, u32 s) { return __builtin_amdgcn_raw_buffer_load_b128(r, v, s, AUX); }
template <int AUX> DI u32x2 ld64(rsrc_t r, u32 v, u32 s) { return __builtin_amdgcn_raw_buffer_load_b64(r, v, s, AUX); }
template <int AUX> DI u32 ld32(rsrc_t r, u32 v, u32 s) { return __builtin_amdgcn_raw_buffer_load_b32(r, v, s, AUX); }
DI void st128(rsrc_t r, u32x4 d, u32 v, u32 s) { __builtin_amdgcn_raw_buffer_store_b128(d, r, v + s, 0, AUX_SC1); }
DI void st32(rsrc_t r, u32 d, u32 v, u32 s) { __builtin_amdgcn_raw_buffer_store_b32(d, r, v, s, AUX_SC1); }
template <int NT = 1> DI void dma(rsrc_t r, u32 v, u32 s, u32 lds) {
  u32 keep;
  if (NT) asm volatile("s_mov_b32 %0, m0\n\ts_mov_b32 m0, %4\n\ts_nop 3\n\tbuffer_load_dwordx4 %1, %2, %3 offen nt lds\n\ts_mov_b32 m0, %0"
                       : "=&s"(keep) : "v"(v), "s"(r), "s"(s), "s"(lds) : "memory");
  else asm volatile("s_mov_b32 %0, m0\n\ts_mov_b32 m0, %4\n\ts_nop 3\n\tbuffer_load_dwordx4 %1, %2, %3 offen lds\n\ts_mov_b32 m0, %0"
                    : "=&s"(keep) : "v"(v), "s"(r), "s"(s), "s"(lds) : "memory");
}
template <int NT = 1> DI void dma4(rsrc_t r, u32 v, u32 lds) {
  u32 keep;
  if (NT) asm volatile("s_mov_b32 %0, m0\n\ts_mov_b32 m0, %3\n\ts_nop 3\n\tbuffer_load_dword %1, %2, 0 offen nt lds\n\ts_mov_b32 m0, %0"
                       : "=&s"(keep) : "v"(v), "s"(r), "s"(lds) : "memory");
  else asm volatile("s_mov_b32 %0, m0\n\ts_mov_b32 m0, %3\n\ts_nop 3\n\tbuffer_load_dword %1, %2, 0 offen lds\n\ts_mov_b32 m0, %0"
                    : "=&s"(keep) : "v"(v), "s"(r), "s"(lds) : "memory");
}
DI void vmwin(int late) {
  asm volatile("s_waitcnt vmcnt(3)\n\ts_cmp_gt_i32 %0, %1\n\ts_cbranch_scc1 1f\n\t"
               "s_waitcnt vmcnt(2)\n\ts_cmp_gt_i32 %0, 0\n\ts_cbranch_scc1 1f\n\t"
               "s_waitcnt vmcnt(1)\n1:" :: "s"(late), "i"(PFAR) : "scc", "memory");
}
#define VMCNT(n) asm volatile("s_waitcnt vmcnt(" #n ")" ::: "memory")
template <int N> DI void vmcnt() { asm volatile("s_waitcnt vmcnt(%0)" ::"i"(N) : "memory"); }
DI void bar() { asm volatile("s_waitcnt lgkmcnt(0)\n\ts_barrier" ::: "memory"); }
#define SCHED() __builtin_amdgcn_sched_barrier(0)

DI u32 pk(float lo, float hi) { f32x2 v = {lo, hi}; return __builtin_bit_cast(u32, __builtin_convertvector(v, bf16x2)); }
DI float blo(u32 u) { return __builtin_bit_cast(float, u << 16); }
DI float bhi(u32 u) { return __builtin_bit_cast(float, u & 0xffff0000u); }
DI f32x4 up4(u32x2 u) { return f32x4{blo(u[0]), bhi(u[0]), blo(u[1]), bhi(u[1])}; }
DI float rb(float x) { return blo(pk(x, x)); }
DI float asf(u32 u) { return __builtin_bit_cast(float, u); }
DI u32 asu(float f) { return __builtin_bit_cast(u32, f); }
DI u32 nosent(u32 u) { return u < SENT - 1u ? u : SENT - 1u; }
template <int CTRL> DI u32 dppu(u32 x) { return __builtin_amdgcn_mov_dpp(x, CTRL, 0xf, 0xf, true); }
template <int CTRL> DI float dppf(float x) { return asf(dppu<CTRL>(asu(x))); }
constexpr int XOR1 = 0xB1, XOR2 = 0x4E, HMIR = 0x141, ROR8 = 0x128;
DI float hi8(float a, float b) { return asf(__builtin_amdgcn_update_dpp(asu(a), asu(b), ROR8, 0xf, 0xc, false)); }
DI float xg_max(float x) {
  auto s = __builtin_amdgcn_permlane16_swap(asu(x), asu(x), false, false);
  x = fmaxf(asf(s[0]), asf(s[1]));
  auto t = __builtin_amdgcn_permlane32_swap(asu(x), asu(x), false, false);
  return fmaxf(asf(t[0]), asf(t[1]));
}
DI float xg_sum(float x) {
  auto s = __builtin_amdgcn_permlane16_swap(asu(x), asu(x), false, false);
  x = asf(s[0]) + asf(s[1]);
  auto t = __builtin_amdgcn_permlane32_swap(asu(x), asu(x), false, false);
  return asf(t[0]) + asf(t[1]);
}
DI float wave_sum(float x) {
  x += dppf<XOR1>(x); x += dppf<XOR2>(x); x += dppf<HMIR>(x); x += dppf<ROR8>(x);
  return xg_sum(x);
}
DI void wave_sum2(float& a, float& b) {
  asm volatile("s_nop 1\n\t"
               "v_add_f32_dpp %0, %0, %0 row_shr:1 row_mask:0xf bank_mask:0xf bound_ctrl:1\n\tv_add_f32_dpp %1, %1, %1 row_shr:1 row_mask:0xf bank_mask:0xf bound_ctrl:1\n\ts_nop 0\n\t"
               "v_add_f32_dpp %0, %0, %0 row_shr:2 row_mask:0xf bank_mask:0xf bound_ctrl:1\n\tv_add_f32_dpp %1, %1, %1 row_shr:2 row_mask:0xf bank_mask:0xf bound_ctrl:1\n\ts_nop 0\n\t"
               "v_add_f32_dpp %0, %0, %0 row_shr:4 row_mask:0xf bank_mask:0xf bound_ctrl:1\n\tv_add_f32_dpp %1, %1, %1 row_shr:4 row_mask:0xf bank_mask:0xf bound_ctrl:1\n\ts_nop 0\n\t"
               "v_add_f32_dpp %0, %0, %0 row_shr:8 row_mask:0xf bank_mask:0xf bound_ctrl:1\n\tv_add_f32_dpp %1, %1, %1 row_shr:8 row_mask:0xf bank_mask:0xf bound_ctrl:1\n\ts_nop 0\n\t"
               "v_add_f32_dpp %0, %0, %0 row_bcast:15 row_mask:0xa bank_mask:0xf\n\tv_add_f32_dpp %1, %1, %1 row_bcast:15 row_mask:0xa bank_mask:0xf\n\ts_nop 0\n\t"
               "v_add_f32_dpp %0, %0, %0 row_bcast:31 row_mask:0xc bank_mask:0xf\n\tv_add_f32_dpp %1, %1, %1 row_bcast:31 row_mask:0xc bank_mask:0xf\n\ts_nop 1"
               : "+v"(a), "+v"(b));
  a = asf(__builtin_amdgcn_readlane(asu(a), 63)); b = asf(__builtin_amdgcn_readlane(asu(b), 63));
}
DI f32x4 mfma(u32x4 a, u32x4 b, f32x4 c) {
  return __builtin_amdgcn_mfma_f32_16x16x32_bf16(__builtin_bit_cast(bf16x8, a), __builtin_bit_cast(bf16x8, b), c, 0, 0, 0);
}
DI u32x2 trd(LAS char* p) { return __builtin_bit_cast(u32x2, __builtin_amdgcn_ds_read_tr16_b64_v4i16((LAS s16x4*)p)); }
DI u32x4 trfrag(LAS char* p, u32 second) { u32x2 a = trd(p), b = trd(p + second); return u32x4{a[0], a[1], b[0], b[1]}; }
DI float silu(float x) { return x * __builtin_amdgcn_rcpf(1.f + __builtin_amdgcn_exp2f(-1.4426950408889634f * x)); }
DI u32 relu2(u32 x) { return __builtin_bit_cast(u32, __builtin_elementwise_max(__builtin_bit_cast(s16x2, x), s16x2{0, 0})); }
DI int rowlane(int L) { return (L & 3) * 16 + (L & 4) * 2 + (L >> 3); }
DI u32 pull(int src, u32 v) { return __builtin_amdgcn_ds_bpermute(src * 4, v); }
DI u32 umax(u32 a, u32 b) { return a > b ? a : b; }
DI u32 mx4(u32 m, u32x4 v) { return umax(umax(umax(m, v[0]), v[1]), umax(v[2], v[3])); }

DI void ln_stats(const float (&v)[12], bool tail, float& mean_b, float& den_b) {
  float s = 0.f;
#pragma unroll
  for (int i = 0; i < 8; ++i) s += v[i];
  float s2 = (v[8] + v[9]) + (v[10] + v[11]);
  s += tail ? s2 : 0.f;
  const float mean = wave_sum(s) * (1.f / DM);
  float q = 0.f, q2 = 0.f;
#pragma unroll
  for (int i = 0; i < 8; ++i) { const float d = v[i] - mean; q = fmaf(d, d, q); }
#pragma unroll
  for (int i = 8; i < 12; ++i) { const float d = v[i] - mean; q2 = fmaf(d, d, q2); }
  q += tail ? q2 : 0.f;
  const float var = wave_sum(q) * (1.f / DM);
  mean_b = rb(mean);
  den_b = rb(__builtin_sqrtf(rb(rb(var) + 1e-5f)));
}

__global__ void __launch_bounds__(NTHR, 3) fused_block(Params p) {
  extern __shared__ __attribute__((aligned(16))) char smem_[];
  LAS char* sm = (LAS char*)smem_;
  const u32 lds0 = (u32)(uintptr_t)sm;
  const int tid = threadIdx.x, lane = tid & 63, w = __builtin_amdgcn_readfirstlane(tid >> 6);
  const int bid = blockIdx.x;
  const u32 tstart = (u32)__builtin_amdgcn_s_memrealtime();
  asm volatile("" : "+s"(p.x), "+s"(p.kc), "+s"(p.vc), "+s"(p.wq), "+s"(p.wo), "+s"(p.w1), "+s"(p.w2), "+s"(p.c), "+s"(p.wm), "+s"(p.out), "+s"(p.ws));
  const int g = lane >> 4, n = lane & 15;
  const int G = (bid >> 5) * 8 + (bid & 7), r4 = (bid >> 3) & 3;
  const bool halfO = G == 25 || G == 26 || G == 61 || G == 63;
  const bool roleM = G < 9, roleO = halfO ? r4 < 2 : G >= 9 && G < 25, roleF = G >= 27 && G < 45, roleQ = G >= 45 && G < 61;
  const int gi = G * 4 + r4;
  const int md0 = gi * 16;
  const int oks = (bid & 7) >> 1, oct = (halfO ? 16 : (bid & 1) * 8 + (bid >> 5 == 2 ? 4 : 0)) + r4;
  const int fj = gi - 108;
  const int qj = gi - 180;
  const rsrc_t rws = mk(p.ws, WS_END), rx = mk(p.x, 8 * DM * 2);
  const u32 frow = ((lane >> 3) & 3) * 8 + (lane >> 5) * 4 + ((lane >> 1) & 3);
  const bool tail = lane < 16;
  const u32 q3 = tail ? lane * 8 + 1024 : 0;

  const rsrc_t rm = mk(p.wm, DM * NMOD * 2);
  if (w == 8) __builtin_amdgcn_s_setprio(3);
  if (w == 8 || roleQ) dma4(rws, WS_EPOCH + (lane & 1) * 4, lds0 + L_EP);
  if (!roleM && w < 2) {
    const int x = bid & 7, u = (bid >> 3) * 42 - 168 + lane;
    if (bid >= 32 && (w == 0 || x == 0)) {
      const u32 v = (lane < 42 && u < 1152) ? (u >> 1) * (NMOD * 2) + (u & 1) * 1152 + (w ? 8 : x) * 128 : 0x7fffffffu;
      dma4<0>(rm, v, lds0 + L_JUNK);
    }
  }
  if (roleM) {
    const rsrc_t rc = mk(p.c, 8 * DM * 2);
    const u32 mv = frow * (NMOD * 2) + (lane & 1) * 16;
    if (w < 5) st128(rws, u32x4{SENT, SENT, SENT, SENT}, g * 256 + n * 16, WS_CLR + (gi * 5 + w) * 4 * 256);
    int wr = w + 2 * r4; wr = wr >= 9 ? wr - 9 : wr;
    u32x2 cv[2], xv[3];
    u32x4 wm[4];
#pragma unroll
    for (int i = 0; i < 3; ++i) xv[i] = ld64<0>(rx, i < 2 ? lane * 8 + i * 512 : q3, (w & 7) * 1152);
#pragma unroll
    for (int i = 0; i < 2; ++i) cv[i] = ld64<0>(rc, (n & 7) * 1152 + g * 16 + (n >> 3) * 8, (2 * wr + i) * 64);
#pragma unroll
    for (int i = 0; i < 4; ++i) wm[i] = ld128<0>(rm, mv, (2 * wr + (i >> 1)) * 32 * NMOD * 2 + ((i & 1) * 576 + md0) * 2);
    SCHED();
    if (w < 8) {
      float v[12];
#pragma unroll
      for (int i = 0; i < 3; ++i) { v[4 * i] = blo(xv[i][0]); v[4 * i + 1] = bhi(xv[i][0]); v[4 * i + 2] = blo(xv[i][1]); v[4 * i + 3] = bhi(xv[i][1]); }
      float mb, db;
      ln_stats(v, tail, mb, db);
      const int q0 = md0 >> 2, qi = q0 >> 6;
      f32x4 o;
#pragma unroll
      for (int k = 0; k < 4; ++k) o[k] = rb(rb((qi == 0 ? v[k] : qi == 1 ? v[4 + k] : v[8 + k]) - mb) / db);
      const int ql = lane - (q0 & 63);
      if (ql >= 0 && ql < 4) *(LAS f32x4*)(sm + L_NX + w * 64 + ql * 16) = o;
    }
    u32x4 bf[2];
#pragma unroll
    for (int i = 0; i < 2; ++i) {
      const u32 a = pk(silu(blo(cv[i][0])), silu(bhi(cv[i][0]))), b = pk(silu(blo(cv[i][1])), silu(bhi(cv[i][1])));
      bf[i] = u32x4{a, b, dppu<ROR8>(a), dppu<ROR8>(b)};
      if (n < 8) *(LAS u32x4*)(sm + L_SC + (2 * wr + i) * 512 + (g * 8 + n) * 16) = bf[i];
    }
    SCHED();
#pragma unroll
    for (int i = 0; i < 4; ++i) *(LAS u32x4*)(sm + L_W + w * 4096 + i * 1024 + lane * 16) = wm[i];
    asm volatile("s_waitcnt vmcnt(0) lgkmcnt(0)" ::: "memory");
    f32x4 dA = {0.f, 0.f, 0.f, 0.f}, dB = dA;
#pragma unroll
    for (int i = 0; i < 2; ++i) {
      LAS char* a = sm + L_W + w * 4096 + i * 2048 + lane * 8;
      dA = mfma(trfrag(a, 512), bf[i], dA);
      dB = mfma(trfrag(a + 1024, 512), bf[i], dB);
    }
    *(LAS f32x4*)(sm + L_RED + wr * 1024 + lane * 16) = f32x4{hi8(dA[0], dB[0]), hi8(dA[1], dB[1]), hi8(dA[2], dB[2]), hi8(dA[3], dB[3])};
  }

  const u32 wv = (g * 8 + ((lane >> 2) & 3)) * 128 + (lane & 3) * 8;
  if (roleQ) {
    const rsrc_t rq = mk(p.wq, DM * NQ * 2);
    const u32 qv = (lane >> 3) * (NQ * 2) + (lane & 7) * 16;
#pragma unroll 1
    for (int i = w * 8; i < w * 8 + 8; ++i) dma(rq, qv, i * 8 * NQ * 2 + qj * 128, lds0 + L_W + i * 1024);
  }
#define QSTAGE() do { \
    u32x4 qw_[8], hg_[2]; \
    _Pragma("unroll") for (int i_ = 0; i_ < 8; ++i_) qw_[i_] = trfrag(sm + L_W + (2 * w + (i_ >> 2)) * 4096 + wv + (i_ & 3) * 32, 512); \
    for (u32 spin_ = 0;; ++spin_) { \
      asm volatile("" ::: "memory"); \
      hg_[0] = ld128<AUX_SC1>(rws, lane * 16, WS_HG + w * 2048); hg_[1] = ld128<AUX_SC1>(rws, lane * 16 + 1024, WS_HG + w * 2048); \
      if (__all(((hg_[0][1] ^ tag0) | (hg_[0][3] ^ tag1) | (hg_[1][1] ^ tag0) | (hg_[1][3] ^ tag1)) == 0) || spin_ > SPIN_CAP) break; \
    } \
    const u32x4 b0_ = {hg_[0][0], hg_[0][2], dppu<ROR8>(hg_[0][0]), dppu<ROR8>(hg_[0][2])}, b1_ = {hg_[1][0], hg_[1][2], dppu<ROR8>(hg_[1][0]), dppu<ROR8>(hg_[1][2])}; \
    const f32x4 z_ = {0.f, 0.f, 0.f, 0.f}; \
    _Pragma("unroll") for (int t_ = 0; t_ < 2; ++t_) { \
      const f32x4 x_ = mfma(qw_[4 + 2 * t_], b1_, mfma(qw_[2 * t_], b0_, z_)), y_ = mfma(qw_[5 + 2 * t_], b1_, mfma(qw_[1 + 2 * t_], b0_, z_)); \
      *(LAS f32x4*)(sm + L_RED + (t_ * 9 + w) * 1024 + lane * 16) = f32x4{hi8(x_[0], y_[0]), hi8(x_[1], y_[1]), hi8(x_[2], y_[2]), hi8(x_[3], y_[3])}; \
    } \
    bar(); } while (0)

  if (w == 8) {
    VMCNT(0);
    const u32x2 ep = *(LAS u32x2*)(sm + L_EP);
    const u32 tag0 = __builtin_amdgcn_readfirstlane(ep[0]) + 1u, tag1 = __builtin_amdgcn_readfirstlane(ep[1]) + 1u;
    if (bid < 8 && lane == 0) st128(rws, u32x4{tstart, tag0, tstart, tag1}, 0, WS_T0 + bid * 16);
    if (roleM) {
      bar();
      f32x4 s = *(LAS f32x4*)(sm + L_RED + lane * 16);
#pragma unroll
      for (int i = 1; i < 9; ++i) s += *(LAS f32x4*)(sm + L_RED + i * 1024 + lane * 16);
      const u32 a = pk(s[0], s[1]), b = pk(s[2], s[3]);
      const u32 ca = dppu<ROR8>(a), cb = dppu<ROR8>(b);
      const f32x4 nx = *(LAS f32x4*)(sm + L_NX + (n & 7) * 64 + g * 16);
      const u32 c0 = pk(1.f + blo(ca), 1.f + bhi(ca)), c1 = pk(1.f + blo(cb), 1.f + bhi(cb));
      const u32 m0 = pk(nx[0] * blo(c0), nx[1] * bhi(c0)), m1 = pk(nx[2] * blo(c1), nx[3] * bhi(c1));
      const u32 h0 = pk(blo(m0) + blo(a), bhi(m0) + bhi(a)), h1 = pk(blo(m1) + blo(b), bhi(m1) + bhi(b));
      if (n < 8) st128(rws, u32x4{h0, tag0, h1, tag1}, (g * 8 + n) * 16, WS_HG + md0 * 32);
    }
    if (roleQ) {
      QSTAGE();
#pragma unroll
      for (int t = 0; t < 2; ++t) {
        f32x4 s = *(LAS f32x4*)(sm + L_RED + t * 9216 + rowlane(lane) * 16);
#pragma unroll
        for (int i = 1; i < 9; ++i) s += *(LAS f32x4*)(sm + L_RED + (t * 9 + i) * 1024 + rowlane(lane) * 16);
        st128(rws, u32x4{pk(s[0], s[1]), tag0, pk(s[2], s[3]), tag1}, (lane >> 3) * 16384 + (t * 8 + (lane & 7)) * 16, WS_QG + qj * 256);
      }
    }
    if (roleF) {
      const rsrc_t r2 = mk(p.w2, DFF * DM * 2);
#pragma unroll 1
      for (int i = 0; i < W2E; ++i) { dma(r2, lane * 16, fj * 73728 + i * 1024, lds0 + L_W2 + i * 1024); vmcnt<SWIN - 1>(); }
    }
    {
      u32x4 qg;
      for (u32 spin = 0;; ++spin) {
        asm volatile("" ::: "memory");
        qg = ld128<AUX_SC1>(rws, lane * 16, WS_QG + (bid >> 1) * 1024);
        if (__all((qg[1] == tag0) & (qg[3] == tag1)) || spin > SPIN_CAP) break;
      }
      *(LAS u32x2*)(sm + L_Q + lane * 8) = u32x2{qg[0], qg[2]};
      if (lane == 0) *(LAS u32x2*)(sm + L_EP + 64) = u32x2{tag0, tag1};
      if (lane == 0) *(LAS u32*)(sm + L_EP + 72) = tstart + PA;
      if (lane < 8) *(LAS u32*)(sm + L_EP + 80 + lane * 4) = 0u;
    }
    bar();
    {
      const u32x4 t0 = ld128<AUX_SC1>(rws, (lane & 7) * 16, WS_T0);
      float a = ((t0[1] == tag0) & (t0[3] == tag1)) ? (float)(int)(t0[0] - tstart) : 0.f, c = 0.f;
      wave_sum2(a, c);
      if (lane == 0) *(volatile LAS u32*)(sm + L_EP + 72) = tstart + (u32)(int)(a * (1.f / 64)) + PA;
    }
    if (roleM) {
      const u32 mv = (lane >> 3) * (NMOD * 2) + (lane & 7) * 16;
#pragma unroll 1
      for (int i = 0; i < 72; ++i) { dma(rm, mv, i * 8 * NMOD * 2 + (1152 + gi * 64) * 2, lds0 + L_W + i * 1024); vmcnt<SWIN - 1>(); }
      VMCNT(0);
      f32x4 d[4];
#pragma unroll
      for (int t = 0; t < 4; ++t) d[t] = f32x4{0.f, 0.f, 0.f, 0.f};
#pragma unroll 1
      for (int s = 0; s < 18; ++s) {
        const u32x4 b = *(LAS u32x4*)(sm + L_SC + s * 512 + (g * 8 + (n & 7)) * 16);
#pragma unroll
        for (int t = 0; t < 4; ++t) d[t] = mfma(trfrag(sm + L_W + s * 4096 + wv + t * 32, 512), b, d[t]);
      }
      const int k9 = gi / 9, kind = k9 == 0 ? 2 : k9 == 3 ? 3 : k9 - 1;
      const int jc = gi - k9 * 9;
      u32 e[4];
#pragma unroll
      for (int t = 0; t < 2; ++t) {
        e[2 * t] = pk(hi8(d[2 * t][0], d[2 * t + 1][0]), hi8(d[2 * t][1], d[2 * t + 1][1]));
        e[2 * t + 1] = pk(hi8(d[2 * t][2], d[2 * t + 1][2]), hi8(d[2 * t][3], d[2 * t + 1][3]));
      }
#pragma unroll
      for (int t = 0; t < 2; ++t) {
        const int src = rowlane((lane & 56) + (lane & 3) * 2 + t);
        const u32 a0 = pull(src, e[0]), a1 = pull(src, e[1]), b0 = pull(src, e[2]), b1 = pull(src, e[3]);
        st128(rws, u32x4{(lane & 4) ? b0 : a0, tag0, (lane & 4) ? b1 : a1, tag1}, (lane >> 3) * 2304 + (lane & 7) * 16,
              WS_MG + kind * 18432 + (jc == 8 ? 2048 + t * 128 : (jc >> 1) * 512 + t * 256 + (jc & 1) * 128));
      }
    }
    if (roleF) {
#pragma unroll 1
      for (int i = 0; i < 9; ++i) st128(rws, u32x4{SENT, SENT, SENT, SENT}, lane * 16, WS_FP + fj * 9216 + i * 1024);
      const rsrc_t r2 = mk(p.w2, DFF * DM * 2);
#pragma unroll 1
      for (int i = W2E; i < 72; ++i) { dma(r2, lane * 16, fj * 73728 + i * 1024, lds0 + L_W2 + i * 1024); vmcnt<SWIN - 1>(); }
    }
    if (roleO) *(LAS u32x2*)(sm + L_A0 + lane * 8) = u32x2{SENT, SENT};
#pragma unroll 1
    for (int c = 0; c < 2; ++c) {
      const u32 org = __builtin_amdgcn_readfirstlane(*(volatile LAS u32*)(sm + L_EP + 72)), tc = org + PB * (c ? CK2 : CK1);
      float a, z = 0.f;
      if (bid >> 3 == 8) {
#pragma unroll 1
        for (int i = 0; i < 512 && (int)((u32)__builtin_amdgcn_s_memrealtime() - tc) < 0; ++i) __builtin_amdgcn_s_sleep(4);
        a = (float)*(volatile LAS int*)(sm + L_EP + 80 + (lane & 7) * 4);
        wave_sum2(a, z);
        if (lane == 0) st128(rws, u32x4{asu(a), tag0, 0u, tag1}, 0, WS_T0 + 256 + c * 256 + (bid & 7) * 16);
      }
#pragma unroll 1
      for (int i = 0; i < 512 && (int)((u32)__builtin_amdgcn_s_memrealtime() - tc) < 130; ++i) __builtin_amdgcn_s_sleep(4);
      const u32x4 t1 = ld128<AUX_SC1>(rws, (lane & 7) * 16, WS_T0 + 256 + c * 256);
      a = ((t1[1] == tag0) & (t1[3] == tag1)) ? asf(t1[0]) : 0.f;
      wave_sum2(a, z);
      const float slip = fminf(fmaxf(a * (1.f / 4096) - (c ? CT2 : CT1), 0.f) * 1.5f, 400.f);
      if (lane == 0) *(volatile LAS u32*)(sm + L_EP + 72) = org + (u32)slip;
    }
    {
      const u32 tpf = __builtin_amdgcn_readfirstlane(*(volatile LAS u32*)(sm + L_EP + 72)) + PB * (16 - RING) - CPF;
#pragma unroll 1
      for (int z = 0; z < 256 && (int)((u32)__builtin_amdgcn_s_memrealtime() - tpf) < 0; ++z) __builtin_amdgcn_s_sleep(8);
      uint64_t pc; u32 len;
      asm volatile("s_getpc_b64 %0\n.Lfb_code_begin:\n\ts_mov_b32 %1, .Lfb_code_end-.Lfb_code_begin" : "=s"(pc), "=s"(len));
      const rsrc_t rcode = mk((const void*)pc, len);
#pragma unroll
      for (int i = 0; i < 3; ++i) dma4<0>(rcode, lane * 64 + i * 4096, lds0 + L_JUNK);
      if (roleF) dma4<0>(mk(p.out, 8 * DM * 2), fj * 128, lds0 + L_JUNK);
    }
  } else {
    const rsrc_t rk = mk((const char*)p.kc + (size_t)(bid >> 1) * 1048576, 1048576);
    const rsrc_t rv = mk((const char*)p.vc + (size_t)(bid >> 1) * 1048576, 1048576);
    const u32 koff = (n & 7) * 256 + (n >> 3) * 64 + g * 16, voff = g * 1024 + n * 16;
    const int rot = (w & 3) + (bid & 1) * 4 + (w >> 2) * 8;
#define TOFF(t) (((t) * 16 + rot) * 4096)
    u32x4 KB[RING][4], VB[RING][4];
#define LOADK(slot, t, W) _Pragma("unroll") for (int i_ = 0; i_ < 4; ++i_) { vmcnt<(W) - 1>(); SCHED(); \
      KB[slot][i_] = ld128<AUX_NT>(rk, koff + (i_ & 1) * 128 + (i_ >> 1) * 2048, TOFF(t)); SCHED(); }
#define LOADV(slot, t, W) _Pragma("unroll") for (int i_ = 0; i_ < 4; ++i_) { vmcnt<(W) - 1>(); SCHED(); \
      VB[slot][i_] = ld128<AUX_NT>(rv, voff + i_ * 256, TOFF(t)); SCHED(); }
    if (roleM) bar();
    if (roleO) {
      const rsrc_t ro = mk(p.wo, NQ * DM * 2);
      const u32 v1 = (g * 8 + ((lane >> 2) & 3)) * (DM * 2) + (lane & 3) * 16;
#pragma unroll 1
      for (int u = w; u < 64; u += 8) { vmcnt<WIN0 - 1>(); dma(ro, v1, (oks * 1024 + (u >> 1) * 32 + (u & 1) * 4) * DM * 2 + oct * 64, lds0 + L_W + u * 1024); }
    }
    if (roleF) {
      const rsrc_t r1 = mk(p.w1, DM * DFF * 2);
      const u32 v1 = (lane >> 3) * (DFF * 2) + (lane & 7) * 16;
#pragma unroll 1
      for (int u = w; u < 72; u += 8) { vmcnt<WIN0 - 1>(); dma(r1, v1, u * 8 * DFF * 2 + fj * 128, lds0 + L_W + u * 1024); }
    }
    LOADK(0, 0, WIN0);
    if (roleQ) {
      VMCNT(0);
      const u32x2 ep = *(LAS u32x2*)(sm + L_EP);
      const u32 tag0 = ep[0] + 1u, tag1 = ep[1] + 1u;
      QSTAGE();
    }
    if (roleQ | roleM) {
      LOADV(0, 0, WINL);
#pragma unroll
      for (int t = 1; t < RING; ++t) { LOADK(t, t, WINL); LOADV(t, t, WINL); }
    } else {
      LOADV(0, 0, WIN0);
#pragma unroll
      for (int t = 1; t < RING; ++t) { LOADK(t, t, WIN0); LOADV(t, t, WIN0); }
    }
    bar();
    u32x4 qf[2];
#pragma unroll
    for (int i = 0; i < 2; ++i) qf[i] = *(LAS u32x4*)(sm + L_Q + (n >> 3) * 256 + i * 128 + (n & 1) * 64 + g * 16);
    u32 pm[4];
#pragma unroll
    for (int i = 0; i < 4; ++i) pm[i] = ((n & 7) >> 1) == i ? ((n & 1) ? 0xffff0000u : 0x0000ffffu) : 0u;
    float am = -INFINITY, al = 0.f, e[4];
    f32x4 acc = {0.f, 0.f, 0.f, 0.f}, s;
    constexpr float SC = 0.08838834764831845f, L2E = 1.4426950408889634f;
#define QK(sl) do { const f32x4 z_ = {0.f, 0.f, 0.f, 0.f}; \
      const f32x4 d1_ = mfma(KB[sl][1], qf[1], mfma(KB[sl][0], qf[0], z_)), d2_ = mfma(KB[sl][3], qf[1], mfma(KB[sl][2], qf[0], z_)); \
      _Pragma("unroll") for (int i_ = 0; i_ < 4; ++i_) { \
        auto x_ = __builtin_amdgcn_permlane32_swap(asu(d1_[i_]), asu(d2_[i_]), false, false); \
        s[i_] = dppf<0xA0>(asf(x_[0])) + dppf<0xF5>(asf(x_[1])); } } while (0)
#define SCORES() do { const u32 a_ = pk(s[0], s[1]), b_ = pk(s[2], s[3]); \
      const u32 a2_ = pk(blo(a_) * SC, bhi(a_) * SC), b2_ = pk(blo(b_) * SC, bhi(b_) * SC); \
      e[0] = blo(a2_); e[1] = bhi(a2_); e[2] = blo(b2_); e[3] = bhi(b2_); } while (0)
#define SOFTMAX() do { SCORES(); \
      const float mn_ = fmaxf(am, xg_max(fmaxf(fmaxf(e[0], e[1]), fmaxf(e[2], e[3])))); \
      const float alpha_ = __builtin_amdgcn_exp2f((am - mn_) * L2E); am = mn_; \
      _Pragma("unroll") for (int i_ = 0; i_ < 4; ++i_) e[i_] = __builtin_amdgcn_exp2f((e[i_] - mn_) * L2E); \
      al = fmaf(al, alpha_, (e[0] + e[1]) + (e[2] + e[3])); acc *= alpha_; } while (0)
#define PV(sl) do { _Pragma("unroll") for (int i_ = 0; i_ < 4; ++i_) { const u32 pb_ = pk(e[i_], e[i_]); \
      acc = mfma(VB[sl][i_], u32x4{pb_ & pm[0], pb_ & pm[1], pb_ & pm[2], pb_ & pm[3]}, acc); } } while (0)
    QK(0); SCHED(); LOADK(0, RING, 64); SOFTMAX(); PV(0); SCHED();
#pragma unroll
    for (int t = 1; t < RING; ++t) { QK(t); SOFTMAX(); PV(t); SCHED(); }
    LOADV(0, RING, WIN);
#define LOADKD(slot, t) _Pragma("unroll") for (int i_ = 0; i_ < 4; ++i_) { vmwin(lv); SCHED(); \
      KB[slot][i_] = ld128<AUX_NT>(rk, koff + (i_ & 1) * 128 + (i_ >> 1) * 2048, TOFF(t)); SCHED(); }
#define LOADVD(slot, t) _Pragma("unroll") for (int i_ = 0; i_ < 4; ++i_) { vmwin(lv); SCHED(); \
      VB[slot][i_] = ld128<AUX_NT>(rv, voff + i_ * 256, TOFF(t)); SCHED(); }
    u32 now = (u32)__builtin_amdgcn_s_memrealtime();
    int lv;
    u32 torg = *(volatile LAS u32*)(sm + L_EP + 72);
#define LATE(t) do { lv = (int)(now - __builtin_amdgcn_readfirstlane(torg)) - PB * ((t) - RING); if (lane == 0) *(volatile LAS int*)(sm + L_EP + 80 + w * 4) = lv; } while (0)
#define STEP(cur, nxt, t) do { LATE(t); \
      LOADKD(nxt, (t) + 1); QK(cur); SOFTMAX(); SCHED(); LOADVD(nxt, (t) + 1); now = (u32)__builtin_amdgcn_s_memrealtime(); torg = *(volatile LAS u32*)(sm + L_EP + 72); SCHED(); PV(cur); SCHED(); } while (0)
    static_assert((16 - RING) % 2 == 1, "pairs plus one");
#pragma unroll 1
    for (int t = RING; t < 15; t += 2) { STEP(0, 1, t); STEP(1, 0, t + 1); }
    QK(0); SOFTMAX(); PV(0);
    al = xg_sum(al);
#pragma unroll
    for (int i = 0; i < 4; ++i) *(LAS float*)(sm + L_AO + w * 1024 + ((n >> 3) * 128 + g * 32 + i * 8 + (n & 7)) * 4) = acc[i];
    if (g == 0 && (n & 7) == 0) *(LAS f32x2*)(sm + L_AM + (n >> 3) * 64 + w * 8) = f32x2{am, al};
  }

  {
    int lane_ = tid & 63;
    asm volatile("" : "+v"(lane_));
    const int lane = lane_, g = lane >> 4, n = lane & 15;
    const int we = w;
    constexpr u32 cap = SPIN_CAP;
    const rsrc_t rst = rws;
    VMCNT(0); bar();
    const u32x2 tg = *(LAS u32x2*)(sm + L_EP + 64);
    const u32 tend = __builtin_amdgcn_readfirstlane(*(LAS u32*)(sm + L_EP + 72)) + PB * (16 - RING);
#define UNTIL(d) do { _Pragma("unroll 1") for (int z_ = 0; z_ < 64 && (int)((u32)__builtin_amdgcn_s_memrealtime() - tend) < (d); ++z_) __builtin_amdgcn_s_sleep(1); } while (0)
    if (we < 3) {
      constexpr float L2E = 1.4426950408889634f;
      __builtin_amdgcn_s_setprio(3);
      const int hh = we < 2 ? we : lane & 1;
      f32x4 ml[4];
#pragma unroll
      for (int i = 0; i < 4; ++i) ml[i] = *(LAS f32x4*)(sm + L_AM + hh * 64 + i * 16);
      f32x2 o[8];
#pragma unroll
      for (int i = 0; i < 8; ++i) o[i] = *(LAS f32x2*)(sm + L_AO + i * 1024 + (we & 1) * 512 + lane * 8);
      float M = ml[0][0];
#pragma unroll
      for (int i = 1; i < 8; ++i) M = fmaxf(M, ml[i >> 1][(i & 1) * 2]);
      float L = 0.f, o0 = 0.f, o1 = 0.f;
#pragma unroll
      for (int i = 0; i < 8; ++i) {
        const float a = __builtin_amdgcn_exp2f((ml[i >> 1][(i & 1) * 2] - M) * L2E);
        L = fmaf(a, ml[i >> 1][(i & 1) * 2 + 1], L); o0 = fmaf(a, o[i][0], o0); o1 = fmaf(a, o[i][1], o1);
      }
      if (we < 2) {
        const float inv = __builtin_amdgcn_rcpf(L);
        const int slot = (((bid >> 1) & 15) * 2 + we) * 16 + (bid & 1) * 8 + (bid >> 5);
        st32(rst, nosent(pk(o0 * inv, o1 * inv)), lane * 4, WS_OP + slot * 256);
      } else if (lane < 16) {
        __builtin_amdgcn_raw_buffer_store_b64(u32x2{asu(fmaf(M, L2E, __builtin_amdgcn_logf(L))), (lane & 1) ? tg[1] : tg[0]}, rst, lane * 8, WS_ML + bid * 128, AUX_SC1);
      }
      __builtin_amdgcn_s_setprio(0);
    }

    if (roleO) {
      if (we < 8) {
        const int hl = (we + oct) & 7;
        u32x4 wf[8], ob[4];
#pragma unroll
        for (int i = 0; i < 8; ++i) wf[i] = trfrag(sm + L_W + (hl * 4 + (i >> 1)) * 2048 + (lane >> 2) * 64 + (i & 1) * 32 + (lane & 3) * 8, 1024);
        const int h0 = oks * 8 + hl;
        u32x4 oa[4];
#pragma unroll
        for (int i = 0; i < 4; ++i) oa[i] = u32x4{SENT, 0u, 0u, 0u};
        UNTIL(ODL);
        for (u32 spin = 0;; ++spin) {
          asm volatile("" ::: "memory");
          bool miss[4];
#pragma unroll
          for (int i = 0; i < 4; ++i) miss[i] = mx4(0u, oa[i]) == SENT;
          if (!__any(miss[0] | miss[1] | miss[2] | miss[3]) || spin >= cap) break;
#pragma unroll
          for (int i = 0; i < 4; ++i) if (miss[i]) oa[i] = ld128<AUX_SC1>(rws, lane * 16 + i * 1024, WS_OP + h0 * 4096);
        }
#pragma unroll
        for (int i = 0; i < 4; ++i) *(LAS u32x4*)(sm + L_OT + we * 4352 + (i * 4 + g) * 272 + n * 16) = oa[i];
#pragma unroll
        for (int i = 0; i < 4; ++i) ob[i] = *(LAS u32x4*)(sm + L_OT + we * 4352 + n * 272 + (i * 4 + g) * 16);
        f32x4 d0 = {0.f, 0.f, 0.f, 0.f}, d1 = d0;
#pragma unroll
        for (int i = 0; i < 4; ++i) { d0 = mfma(wf[2 * i], ob[i], d0); d1 = mfma(wf[2 * i + 1], ob[i], d1); }
        u32 au;
        for (u32 spin = 0;; ++spin) {
          au = *(volatile LAS u32*)(sm + L_A0 + hl * 64 + n * 4);
          if (!__any(au == SENT) || spin >= cap * 16) break;
        }
        const float a0 = asf(au);
        d0 *= a0; d1 *= a0;
        f32x4 e;
#pragma unroll
        for (int i = 0; i < 4; ++i) e[i] = (n < 8 ? d0[i] : d1[i]) + dppf<ROR8>(n < 8 ? d1[i] : d0[i]);
        *(LAS f32x4*)(sm + L_RED + hl * 1024 + lane * 16) = e;
      } else {
        u32x4 ls = {0u, ~tg[0], 0u, 0u};
        UNTIL(ODL);
        for (u32 spin = 0;; ++spin) {
          asm volatile("" ::: "memory");
          if ((ls[1] != tg[0]) | (ls[3] != tg[1])) ls = ld128<AUX_SC1>(rws, (((n & 7) * 16 + g) * 2 + (n >> 3)) * 128, WS_ML + oks * 1024);
          if (__all((ls[1] == tg[0]) & (ls[3] == tg[1])) || spin >= cap) break;
          __builtin_amdgcn_s_sleep(2);
        }
#pragma unroll
        for (int j = 0; j < 2; ++j)
          *(LAS u32*)(sm + L_A0 + (g * 2 + j) * 64 + n * 4) = nosent(asu(__builtin_amdgcn_rcpf(1.f + __builtin_amdgcn_exp2f(dppf<ROR8>(asf(ls[2 * j])) - asf(ls[2 * j])))));
      }
      bar();
      if (we == 0) {
        f32x4 s = *(LAS f32x4*)(sm + L_RED + rowlane(lane) * 16);
#pragma unroll
        for (int i = 1; i < 8; ++i) s += *(LAS f32x4*)(sm + L_RED + i * 1024 + rowlane(lane) * 16);
        __builtin_amdgcn_raw_buffer_store_b64(u32x2{nosent(pk(s[0], s[1])), nosent(pk(s[2], s[3]))}, rst, (lane >> 3) * 5 * 256 + (lane & 7) * 8,
                                              WS_X2 + (oks * 40 + (oct >> 2)) * 256 + (oct & 3) * 64, AUX_SC1);
      }
    }

    if (roleF) {
      const int jb = fj / 9, jc = fj - jb * 9, wr = (we + r4 * 2 + (bid >> 5)) & 7;
      u32x4 w1f[9], w2f[8], sel;
#pragma unroll
      for (int i = 0; i < 4; ++i) sel[i] = ((n & 7) >> 1) == i ? ((n & 1) ? 0x3f800000u : 0x00003f80u) : 0u;
      const bool t3 = g < 2 && n < 8;
      const u32 ql = (g >> 1) * 32 + n * 2 + (g & 1);
      f32x4 xv[3], ga[3], sb[3], cb[3];
      if (we < 8) {
        u32x4 mg[9];
        for (u32 spin = 0;; ++spin) {
          asm volatile("" ::: "memory");
          u32 bad = 0;
#pragma unroll
          for (int i = 0; i < 9; ++i) mg[i] = ld128<AUX_SC1>(rws, (i % 3 < 2 ? lane + (i % 3) * 64 : 128 + (g & 1) * 8 + (n & 7)) * 16, WS_MG + (i / 3) * 18432 + wr * 2304);
#pragma unroll
          for (int i = 0; i < 9; ++i) bad |= (mg[i][1] ^ tg[0]) | (mg[i][3] ^ tg[1]);
          if (__all(bad == 0) || spin >= cap) break;
        }
#pragma unroll
        for (int i = 0; i < 3; ++i) {
          const u32x2 a = ld64<0>(rx, (i < 2 ? ql + i * 64 : t3 ? ql + 128 : 0) * 8, wr * 1152);
          xv[i] = f32x4{blo(a[0]), bhi(a[0]), blo(a[1]), bhi(a[1])};
          sb[i] = up4(u32x2{mg[i][0], mg[i][2]});
          cb[i] = up4(u32x2{pk(1.f + blo(mg[3 + i][0]), 1.f + bhi(mg[3 + i][0])), pk(1.f + blo(mg[3 + i][2]), 1.f + bhi(mg[3 + i][2]))});
          ga[i] = f32x4{blo(mg[6 + i][0]), bhi(mg[6 + i][0]), blo(mg[6 + i][2]), bhi(mg[6 + i][2])};
          if (i == 2 && !t3) xv[i] = ga[i] = f32x4{0.f, 0.f, 0.f, 0.f};
          asm volatile("" : "+v"(xv[i]), "+v"(sb[i]), "+v"(cb[i]), "+v"(ga[i]));
        }
      }
      SCHED();
#pragma unroll
      for (int i = 0; i < 9; ++i) w1f[i] = trfrag(sm + L_W + (((we >> 2) & 1) * 9 + i) * 4096 + (g * 8 + ((lane >> 2) & 3)) * 128 + (we & 3) * 32 + (lane & 3) * 8, 512);
#pragma unroll
      for (int i = 0; i < 8; ++i) w2f[i] = trfrag(sm + L_W2 + ((i >> 2) * 32 + (lane >> 2)) * 1152 + (4 * we + (i & 3)) * 32 + (lane & 3) * 8, 16 * 1152);
      const u32 oq = ((n >> 2) & 1) * 8 + (g & 1) * 4 + (n & 3);
      if (we == 8) {
        for (u32 spin = 0;; ++spin) {
          asm volatile("" ::: "memory");
          const u32x4 m = ld128<AUX_SC1>(rws, (oq >> 1) * 16 + (oq & 1) * (jc == 8 ? 128 : 256), WS_MG + 3 * 18432 + jb * 2304 + (jc >> 1) * 512 + (jc & 1) * 128);
          *(LAS u32x2*)(sm + L_EP + 128 + oq * 8) = u32x2{m[0], m[2]};
          if (__all((m[1] == tg[0]) & (m[3] == tg[1])) || spin >= cap) break;
        }
        for (u32 spin = 0;; ++spin) {
          asm volatile("" ::: "memory");
          const u32 a = ld32<AUX_SC1>(rws, (lane & 3) * 40 * 256, WS_X2 + (jb * 5 + (jc >> 1)) * 256 + (jc & 1) * 128);
          if (__any(a != SENT) || spin >= cap) break;
          __builtin_amdgcn_s_sleep(1);
        }
      }
      bar();
      if (we < 8) {
        u32x4 pa[5];
        const u32x4 z4 = {0u, 0u, 0u, 0u};
        u32x4 selL = n < 8 ? sel : z4, selH = n < 8 ? z4 : sel;
        asm volatile("" : "+v"(selL), "+v"(selH));
        for (u32 spin = 0;; ++spin) {
          asm volatile("" ::: "memory");
#pragma unroll
          for (int i = 0; i < 5; ++i) pa[i] = ld128<AUX_SC1>(rws, g * 40 * 256 + (i < 4 ? n : n & 7) * 16, WS_X2 + (wr * 5 + i) * 256);
          u32 m = 0;
#pragma unroll
          for (int i = 0; i < 5; ++i) m = mx4(m, pa[i]);
          if (!__any(m == SENT) || spin >= cap) break;
        }
        const f32x4 z = {0.f, 0.f, 0.f, 0.f};
        f32x4 v[3];
        v[0] = mfma(selH, pa[1], mfma(selL, pa[0], z)); v[1] = mfma(selH, pa[3], mfma(selL, pa[2], z)); v[2] = mfma(selL, pa[4], z);
#pragma unroll
        for (int i = 0; i < 3; ++i) v[i] = xv[i] + ga[i] * v[i];
        const f32x4 s1 = v[0] + v[1] + v[2], s2 = v[0] * v[0] + v[1] * v[1] + v[2] * v[2];
        float md = (s1[0] + s1[1]) + (s1[2] + s1[3]), sq = (s2[0] + s2[1]) + (s2[2] + s2[3]);
        wave_sum2(md, sq);
        md *= 1.f / DM;
        const float rs = __builtin_amdgcn_rsqf(fmaxf(fmaf(sq, 1.f / DM, -md * md), 0.f) + 1e-5f), mo = -md * rs;
        const f32x4 h0 = (v[0] * rs + mo) * cb[0] + sb[0], h1 = (v[1] * rs + mo) * cb[1] + sb[1], h2 = (v[2] * rs + mo) * cb[2] + sb[2];
        {
          *(LAS u32x2*)(sm + L_H2 + wr * H2S + ql * 8) = u32x2{pk(h0[0], h0[1]), pk(h0[2], h0[3])};
          *(LAS u32x2*)(sm + L_H2 + wr * H2S + ql * 8 + 512) = u32x2{pk(h1[0], h1[1]), pk(h1[2], h1[3])};
          if (t3) *(LAS u32x2*)(sm + L_H2 + wr * H2S + ql * 8 + 1024) = u32x2{pk(h2[0], h2[1]), pk(h2[2], h2[3])};
        }
        if (wr == jb) {
          const int qi = jc >> 2, qq = (int)ql - (jc & 3) * 16;
          if (qq >= 0 && qq < 16) *(LAS f32x4*)(sm + L_XR + qq * 16) = qi == 0 ? v[0] : qi == 1 ? v[1] : v[2];
        }
      }
      bar();
      if (we < 8) {
        f32x4 d = {0.f, 0.f, 0.f, 0.f};
#pragma unroll
        for (int i = 0; i < 9; ++i) d = mfma(w1f[i], *(LAS u32x4*)(sm + L_H2 + (n & 7) * H2S + ((we >> 2) * 9 + i) * 64 + g * 16), d);
        if (n < 8) *(LAS f32x4*)(sm + L_H1 + (we & 3) * 1024 + (we >> 2) * 512 + (g * 8 + n) * 16) = d;
      }
      bar();
      {
        u32x4 hb[2];
#pragma unroll
        for (int t = 0; t < 4; ++t) {
          f32x4 s = *(LAS f32x4*)(sm + L_H1 + t * 1024 + (n >> 3) * 512 + (g * 8 + (n & 7)) * 16);
#pragma unroll
          for (int i = 0; i < 4; ++i) s[i] += dppf<ROR8>(s[i]);
          hb[t >> 1][(t & 1) * 2] = relu2(pk(s[0], s[1])); hb[t >> 1][(t & 1) * 2 + 1] = relu2(pk(s[2], s[3]));
        }
        const f32x4 z = {0.f, 0.f, 0.f, 0.f};
        f32x4 e[4];
#pragma unroll
        for (int i = 0; i < 4; ++i) e[i] = mfma(w2f[4 + i], hb[1], mfma(w2f[i], hb[0], z));
        const f32x4 e0 = n < 8 ? e[0] : e[2], e1 = n < 8 ? e[1] : e[3];
        const int src = rowlane(lane);
        st128(rst, u32x4{pull(src, nosent(pk(e0[0], e0[1]))), pull(src, nosent(pk(e0[2], e0[3]))), pull(src, nosent(pk(e1[0], e1[1]))), pull(src, nosent(pk(e1[2], e1[3])))},
              (lane >> 3) * (9 * 9216) + (lane & 7) * 16, WS_FP + we * 9216 + fj * 128);
      }
      if (we == 8) {
        const f32x4 xr = *(LAS f32x4*)(sm + L_XR + oq * 16);
        const u32x2 gb = *(LAS u32x2*)(sm + L_EP + 128 + oq * 8);
        u32x4 fa[9], sel2;
        int n2 = n;
        asm volatile("" : "+v"(n2));
#pragma unroll
        for (int i = 0; i < 4; ++i) sel2[i] = ((n2 & 7) >> 1) == i ? ((n2 & 1) ? 0x3f800000u : 0x00003f80u) : 0u;
        __builtin_amdgcn_s_sleep(20);
        for (u32 spin = 0;; ++spin) {
          asm volatile("" ::: "memory");
          u32 m = 0;
#pragma unroll
          for (int i = 0; i < 9; ++i) fa[i] = ld128<AUX_SC1>(rws, lane * 16 + i * 1024, WS_FP + fj * 9216);
#pragma unroll
          for (int i = 0; i < 9; ++i) m = mx4(m, fa[i]);
          if (!__any(m == SENT) || spin >= cap) break;
        }
        f32x4 y = {0.f, 0.f, 0.f, 0.f};
#pragma unroll
        for (int i = 0; i < 9; ++i) y = mfma(sel2, fa[i], y);
#pragma unroll
        for (int i = 0; i < 4; ++i) y[i] += dppf<ROR8>(y[i]);
        const rsrc_t ro = mk(p.out, 8 * DM * 2);
        if (t3) __builtin_amdgcn_raw_buffer_store_b64(u32x2{pk(fmaf(blo(gb[0]), y[0], xr[0]), fmaf(bhi(gb[0]), y[1], xr[1])),
                                                            pk(fmaf(blo(gb[1]), y[2], xr[2]), fmaf(bhi(gb[1]), y[3], xr[3]))}, ro, oq * 8, fj * 128, AUX_SC1);
        if (fj == 0 && lane == 0) __builtin_amdgcn_raw_buffer_store_b64(tg, rst, 0, WS_EPOCH, AUX_SC1);
      }
    }
  }
  asm volatile(".Lfb_code_end:");
}

extern "C" void kernel_launch(void* const* d_in, const int* in_sizes, int n_in, void* d_out, int out_size, void* d_ws, size_t ws_size,
                              hipStream_t stream) {
  Params p;
  p.x = d_in[0]; p.kc = d_in[1]; p.vc = d_in[2]; p.wq = d_in[3]; p.wo = d_in[4];
  p.w1 = d_in[6]; p.w2 = d_in[7]; p.c = d_in[8]; p.wm = d_in[9]; p.out = d_out; p.ws = (char*)d_ws;
  (void)hipFuncSetAttribute((const void*)fused_block, hipFuncAttributeMaxDynamicSharedMemorySize, L_END);
  hipLaunchKernelGGL(fused_block, dim3(NWG), dim3(NTHR), L_END, stream, p);
}
```
